```python
import torch

def _softmax(x, axis=-1):
    return torch.softmax(x, dim=axis)
def _gelu(x):
    return torch.nn.functional.gelu(x, approximate="tanh")
def _silu(x):
    return torch.nn.functional.silu(x)
def _sigmoid(x):
    return torch.sigmoid(x)
def _layernorm(x, axis=-1, eps=1e-5):
    m = x.mean(dim=axis, keepdim=True)
    v = x.var(dim=axis, keepdim=True, unbiased=False)
    return (x - m) / torch.sqrt(v + eps)
def _scatter_add(tgt, idx, src, axis=0):
    return tgt.clone().index_add_(axis, idx, src)
def _segment_sum(src, seg, n):
    out = torch.zeros((n,) + src.shape[1:], dtype=src.dtype)
    return out.index_add_(0, seg, src)
def _pad(x, pw):
    flat = tuple(v for pair in reversed(pw) for v in pair)
    return torch.nn.functional.pad(x, flat)

kernel_name = 'sequential_preLN__sliding_block__moe_arch_mod__prompt'

def setup_inputs():

    _w = (lambda a: (lambda t: t.to(torch.bfloat16) if t.is_floating_point() else t)(torch.as_tensor(a)))
    def _A_setup():
        B, S, D = 1, 2048, 512
        return {
            "x": _w((lambda *s: torch.randn(*s, dtype=torch.bfloat16) * 0.05)(B, S, D)),
            "wqkv": _w((lambda *s: torch.randn(*s, dtype=torch.bfloat16) * 0.05)(D, 3 * D)),
            "wo": _w((lambda *s: torch.randn(*s, dtype=torch.bfloat16) * 0.05)(D, D)),
        }
    _A_in = _A_setup()

    def _B_setup():
        B, S, D, F, C = 1, 2048, 512, 1376, 2
        return {
            "x": _w((lambda *s: torch.randn(*s, dtype=torch.bfloat16) * 0.05)(B, S, D)),
            "w_r": _w((lambda *s: torch.randn(*s, dtype=torch.bfloat16) * 0.05)(D)),
            "w_gate": _w((lambda *s: torch.randn(*s, dtype=torch.bfloat16) * 0.05)(D, F)),
            "w_up": _w((lambda *s: torch.randn(*s, dtype=torch.bfloat16) * 0.05)(D, F)),
            "w_down": _w((lambda *s: torch.randn(*s, dtype=torch.bfloat16) * 0.05)(F, D)),
        }
    _B_in = _B_setup()

    return {
        "A_x": _A_in["x"],
        "A_wqkv": _A_in["wqkv"],
        "A_wo": _A_in["wo"],
        "B_x": _B_in["x"],
        "B_w_r": _B_in["w_r"],
        "B_w_gate": _B_in["w_gate"],
        "B_w_up": _B_in["w_up"],
        "B_w_down": _B_in["w_down"],
    }

def reference(A_x, A_wqkv, A_wo, B_x, B_w_r, B_w_gate, B_w_up, B_w_down):
    def _A_ref(x, wqkv, wo):
        B, S, D = x.shape
        H = 32; Dh = D // H; W = 256
        qkv = (lambda x, *d: x.permute(*d))((x @ wqkv).reshape(B, S, 3, H, Dh), 2, 0, 3, 1, 4)
        q, k, v = qkv[0], qkv[1], qkv[2]
        scores = torch.einsum("bhqd,bhkd->bhqk", q, k) * (Dh ** -0.5)
        idx = torch.arange(S)
        mask = (idx[:, None] - idx[None, :] >= 0) & (idx[:, None] - idx[None, :] < W)
        scores = torch.where(mask[None, None], scores, -1e9)
        p = _softmax(scores, axis=-1)
        o = torch.einsum("bhqk,bhkd->bhqd", p, v)
        return (lambda x, *d: x.permute(*d))(o, 0, 2, 1, 3).reshape(B, S, D) @ wo

    def _B_ref(x, w_r, w_gate, w_up, w_down):
        B, S, D = x.shape
        C = 2
        r = x @ w_r
        idx = torch.argsort(-r, axis=-1)[:, :C]
        idx_e = (lambda x, n, axis: torch.repeat_interleave(x, n, dim=axis))(idx.reshape(B, C, 1), D, axis=2)
        x_sel = (lambda x, i, axis: torch.take_along_dim(x, i, dim=axis))(x, idx_e, axis=1)
        g = _silu(x_sel @ w_gate)
        u = x_sel @ w_up
        y = (g * u) @ w_down
        r_sel = _sigmoid((lambda x, i, axis: torch.take_along_dim(x, i, dim=axis))(r, idx, axis=1))
        y = y * r_sel.reshape(B, C, 1)
        b_off = (torch.arange(B) * S).reshape(B, 1)
        flat_idx = (idx + b_off).reshape(B * C)
        zero = (x * 0.0).reshape(B * S, D)
        upd = _scatter_add(zero, flat_idx, y.reshape(B * C, D), axis=0)
        return x + upd.reshape(B, S, D)

    x = A_x
    h = x + _A_ref(_layernorm(x, axis=-1), A_wqkv, A_wo)
    h2 = h + _B_ref(_layernorm(h, axis=-1), B_w_r, B_w_gate, B_w_up, B_w_down)
    return h2
```

```cpp
#include <hip/hip_runtime.h>
#include <hip/hip_bf16.h>

typedef unsigned long long u64;
typedef unsigned v4u __attribute__((ext_vector_type(4)));
typedef unsigned v2u __attribute__((ext_vector_type(2)));
typedef float f32x2 __attribute__((ext_vector_type(2)));
typedef float f32x4 __attribute__((ext_vector_type(4)));
typedef float f32x16 __attribute__((ext_vector_type(16)));
typedef __bf16 bf16x2 __attribute__((ext_vector_type(2)));
typedef __bf16 bf16x4 __attribute__((ext_vector_type(4)));
typedef __bf16 bf16x8 __attribute__((ext_vector_type(8)));
#define GAS __attribute__((address_space(1)))
#define LAS __attribute__((address_space(3)))
#define DI __device__ __forceinline__
#define RLX_AGENT __ATOMIC_RELAXED, __HIP_MEMORY_SCOPE_AGENT

#define STAMPS 0

namespace {
constexpr int FF = 1376;
constexpr int AUX_SC1 = 16;
constexpr unsigned SPIN_CAP = 1u << 16;
constexpr unsigned WS_EPOCH = 0;
constexpr unsigned WS_KVFLAG = 4096;
constexpr unsigned WS_OFLAG = WS_KVFLAG + 256 * 128;
constexpr unsigned WS_KEYS = WS_OFLAG + 256 * 128;
constexpr unsigned WS_AGRAN = WS_KEYS + 8 * 2048;
constexpr unsigned WS_STATS = WS_AGRAN + 192 * 64;
constexpr unsigned WS_KV = 1u << 20;
constexpr unsigned WS_O = WS_KV + 256 * 16384;
constexpr unsigned WS_HROWS = WS_O + 128 * 16384;
constexpr unsigned WS_STAMP = WS_HROWS + 2048 * 4096;
constexpr unsigned WS_BYTES = WS_STAMP + 256 * 8 * 32 * 8;
static_assert(WS_STATS + 64 * 4096 <= WS_KV, "ws map");
constexpr int XROW = 1040;
constexpr int L_X = 0;
constexpr int L_RING = 64 * XROW;
constexpr int L_MISC = L_RING + 5 * 16384;
constexpr int L_XS = L_MISC;
constexpr int L_CS = L_XS + 2048;
constexpr int L_CSQ = L_CS + 1024;
constexpr int L_TAB = L_CSQ + 1024;
constexpr int L_SCR = L_TAB + 1024;
constexpr int L_EX = L_RING + 2 * 16384;
constexpr int KVREC = 4096 + 8 * 528;
constexpr int L_OST = 10 * KVREC;
constexpr int L_FFW = L_OST + 32768;
constexpr int L_P3 = L_FFW + 44032;
constexpr int L_TOTAL = 163840;
static_assert(L_SCR + 8192 <= L_TOTAL && L_P3 + 3584 <= L_TOTAL, "lds map");

struct Params {
  const char* x; const char* wqkv; const char* wo; const char* wr; const char* wg; const char* wu; const char* wd;
  char* out; char* ws;
};

DI float asf(unsigned u) { return __builtin_bit_cast(float, u); }
DI unsigned asu(float f) { return __builtin_bit_cast(unsigned, f); }
DI float bflo(unsigned u) { return __builtin_bit_cast(float, u << 16); }
DI float bfhi(unsigned u) { return __builtin_bit_cast(float, u & 0xffff0000u); }
DI unsigned pk2(float a, float b) { f32x2 f = {a, b}; return __builtin_bit_cast(unsigned, __builtin_convertvector(f, bf16x2)); }
DI float rbf(float a) { return bflo(pk2(a, a)); }
DI float dot2(unsigned a, unsigned b, float c) {
  return __builtin_amdgcn_fdot2_f32_bf16(__builtin_bit_cast(bf16x2, a), __builtin_bit_cast(bf16x2, b), c, false);
}
DI f32x16 mfma32(v4u a, v4u b, f32x16 c) {
  return __builtin_amdgcn_mfma_f32_32x32x16_bf16(__builtin_bit_cast(bf16x8, a), __builtin_bit_cast(bf16x8, b), c, 0, 0, 0);
}
DI f32x4 mfma16(v4u a, v4u b, f32x4 c) {
  return __builtin_amdgcn_mfma_f32_16x16x32_bf16(__builtin_bit_cast(bf16x8, a), __builtin_bit_cast(bf16x8, b), c, 0, 0, 0);
}
DI float sum32(float x) {
  const unsigned u = __builtin_bit_cast(unsigned, x);
  auto r = __builtin_amdgcn_permlane32_swap(u, u, false, false);
  const unsigned r0 = r[0], r1 = r[1];
  return __builtin_bit_cast(float, r0) + __builtin_bit_cast(float, r1);
}
DI float sum16(float x) {
  const unsigned u = __builtin_bit_cast(unsigned, x);
  auto r = __builtin_amdgcn_permlane16_swap(u, u, false, false);
  const unsigned r0 = r[0], r1 = r[1];
  return __builtin_bit_cast(float, r0) + __builtin_bit_cast(float, r1);
}
DI float fmax32(float x) {
  const unsigned u = __builtin_bit_cast(unsigned, x);
  auto r = __builtin_amdgcn_permlane32_swap(u, u, false, false);
  const unsigned r0 = r[0], r1 = r[1];
  return fmaxf(__builtin_bit_cast(float, r0), __builtin_bit_cast(float, r1));
}
DI unsigned umax_row16(unsigned k) {
  k = max(k, (unsigned)__builtin_amdgcn_update_dpp(0, (int)k, 0xB1, 0xF, 0xF, true));
  k = max(k, (unsigned)__builtin_amdgcn_update_dpp(0, (int)k, 0x4E, 0xF, 0xF, true));
  k = max(k, (unsigned)__builtin_amdgcn_update_dpp(0, (int)k, 0x141, 0xF, 0xF, true));
  k = max(k, (unsigned)__builtin_amdgcn_update_dpp(0, (int)k, 0x140, 0xF, 0xF, true));
  return k;
}
DI unsigned umax_wave(unsigned k) {
  k = umax_row16(k);
  { auto r = __builtin_amdgcn_permlane16_swap(k, k, false, false); const unsigned r0 = r[0], r1 = r[1]; k = max(r0, r1); }
  { auto r = __builtin_amdgcn_permlane32_swap(k, k, false, false); const unsigned r0 = r[0], r1 = r[1]; k = max(r0, r1); }
  return k;
}
DI float fsum_row16(float v) {
  v += __builtin_bit_cast(float, __builtin_amdgcn_update_dpp(0, __builtin_bit_cast(int, v), 0xB1, 0xF, 0xF, true));
  v += __builtin_bit_cast(float, __builtin_amdgcn_update_dpp(0, __builtin_bit_cast(int, v), 0x4E, 0xF, 0xF, true));
  v += __builtin_bit_cast(float, __builtin_amdgcn_update_dpp(0, __builtin_bit_cast(int, v), 0x141, 0xF, 0xF, true));
  v += __builtin_bit_cast(float, __builtin_amdgcn_update_dpp(0, __builtin_bit_cast(int, v), 0x140, 0xF, 0xF, true));
  return v;
}
DI void glds16(const char* base, unsigned voff, unsigned ldsoff) {
  unsigned keep;
  asm volatile("s_mov_b32 %0, m0\n\ts_mov_b32 m0, %3\n\ts_nop 0\n\tglobal_load_lds_dwordx4 %1, %2\n\ts_mov_b32 m0, %0"
               : "=&s"(keep) : "v"(voff), "s"(base), "s"(ldsoff) : "memory");
}
#define WAIT_VM(n) asm volatile("s_waitcnt vmcnt(" #n ")" ::: "memory")
#define BAR() asm volatile("s_waitcnt lgkmcnt(0)\n\ts_barrier" ::: "memory")
#define LDS_U4(off) (*(LAS v4u*)(size_t)(off))
#define LDS_F4(off) (*(LAS f32x4*)(size_t)(off))
#define LDS_F2(off) (*(LAS f32x2*)(size_t)(off))
#define LDS_U2(off) (*(LAS v2u*)(size_t)(off))
#define LDS_F1(off) (*(LAS float*)(size_t)(off))
#define LDS_U1(off) (*(LAS unsigned*)(size_t)(off))
DI v2u tr16(unsigned off) { return __builtin_bit_cast(v2u, __builtin_amdgcn_ds_read_tr16_b64_v4bf16((LAS bf16x4*)(size_t)off)); }
DI v4u tr16x2(unsigned off, unsigned d) { const v2u a = tr16(off), b = tr16(off + d); return (v4u){a.x, a.y, b.x, b.y}; }

#define STAMP(i) do {} while (0)

__global__ __launch_bounds__(512, 2) void fused_block_kernel(Params p) {
  extern __shared__ __attribute__((aligned(16))) char smem[];
  const int tid = threadIdx.x, lane = tid & 63;
  const int w = __builtin_amdgcn_readfirstlane(tid >> 6);
  const int b = blockIdx.x, xcd = b & 7, slot = b >> 3;
  const int tt = (xcd & 3) * 8 + (slot & 7), gg = (xcd >> 2) * 4 + (slot >> 3);
  const unsigned sb = (unsigned)(size_t)(LAS char*)smem;
  const __amdgpu_buffer_rsrc_t rws = __builtin_amdgcn_make_buffer_rsrc(p.ws, 0, WS_BYTES, 0x00020000);
  const int r32 = lane & 31, h = lane >> 5;
  const unsigned tag = *(const GAS unsigned*)(p.ws + WS_EPOCH) + 1u;
  STAMP(0);

  const int wrow = tid >> 3;
  const unsigned wvoff = (unsigned)(wrow * 3072 + gg * 128 + (((tid & 7) ^ (((wrow >> 1) & 1) << 2)) << 4));
  const int rot = tt & 7;
  glds16(p.wqkv + (size_t)(rot * 64) * 3072, wvoff, sb + L_SCR + w * 1024);
  {
    const char* xb = p.x + (size_t)(tt * 64 + w) * 1024;
#pragma unroll
    for (int i = 0; i < 8; ++i) glds16(xb + i * 8192, (unsigned)lane * 16, sb + L_X + (i * 8 + w) * XROW);
  }
  auto issue = [&](int i) {
    const unsigned dst = sb + L_RING + (i % 5) * 16384 + w * 1024;
    const char* s0; const char* s1;
    if (i < 8) { s0 = p.wqkv + (size_t)(((i + rot) & 7) * 64) * 3072 + 1024; s1 = s0 + 1024; }
    else { s0 = p.wqkv + (size_t)(((i + rot) & 3) * 64) * 3072; s1 = s0 + (size_t)256 * 3072; }
    glds16(s0, wvoff, dst);
    glds16(s1, wvoff, dst + 8192);
  };
#pragma unroll
  for (int i = 0; i < 5; ++i) issue(i);

  const int tb = w & 1, ct = (w >> 1) & 1, hi = w >> 2;
  const int role = (w >> 1) ^ (tb << 1);
  unsigned xa[4], wa[4];
  {
    const int q = (lane & 15) >> 2, pp = lane & 3, blk = (lane >> 4) & 1;
    const unsigned x0 = sb + L_X + (tb * 32 + r32) * XROW + h * 16;
    const unsigned w0 = sb + L_RING + hi * 8192 + (8 * h + q) * 128 + ((ct ^ ((q >> 1) & 1)) << 6) + blk * 32 + pp * 8;
#pragma unroll
    for (int i = 0; i < 4; ++i) { xa[i] = x0 + (i ^ role) * 32; wa[i] = w0 + (i ^ role) * 2048; }
  }
  constexpr unsigned ONE2 = 0x3F803F80u;
  f32x16 acc;
#pragma unroll
  for (int i = 0; i < 16; ++i) acc[i] = 0.f;
  float xs = 0.f, xss = 0.f, cs = 0.f;
  auto chunk = [&](unsigned xoff, unsigned woff, bool swap, bool stats) {
    v4u xf[4], wf[4];
#pragma unroll
    for (int i = 0; i < 4; ++i) { xf[i] = LDS_U4(xa[i] + xoff); wf[i] = tr16x2(wa[i] + woff, 512); }
    if (swap) {
#pragma unroll
      for (int i = 0; i < 4; ++i) acc = mfma32(xf[i], wf[i], acc);
    } else {
#pragma unroll
      for (int i = 0; i < 4; ++i) acc = mfma32(wf[i], xf[i], acc);
    }
    if (stats) {
#pragma unroll
      for (int j = 0; j < 4; ++j) { xs = dot2(xf[0][j], ONE2, xs); xss = dot2(xf[0][j], xf[0][j], xss); }
    }
#pragma unroll
    for (int j = 0; j < 4; ++j) { cs = dot2(wf[0][j], ONE2, cs); cs = dot2(wf[1][j], ONE2, cs); }
  };
  {
    int sl = 0;
    for (int i = 0; i < 8; ++i) {
      WAIT_VM(6);
      BAR();
      if (i >= 1) {
        const int n = i + 4;
        const int psl = sl == 0 ? 4 : sl - 1;
        const unsigned dst = sb + L_RING + psl * 16384 + w * 1024;
        const char* s0; const char* s1;
        if (n < 8) { s0 = p.wqkv + (size_t)(((n + rot) & 7) * 64) * 3072 + 1024; s1 = s0 + 1024; }
        else { s0 = p.wqkv + (size_t)(((n + rot) & 3) * 64) * 3072; s1 = s0 + (size_t)256 * 3072; }
        glds16(s0, wvoff, dst);
        glds16(s1, wvoff, dst + 8192);
      }
      chunk(((i + rot) & 7) * 128, sl * 16384, hi != 0, true);
      sl = sl == 4 ? 0 : sl + 1;
    }
  }
  STAMP(1);
  xs = sum32(xs); xss = sum32(xss); cs = sum32(cs);
  if (h == 0) {
    LDS_F2(sb + L_XS + ((tb * 4 + (w >> 1)) * 32 + r32) * 8) = (f32x2){xs, xss};
    LDS_F1(sb + L_CS + (((w >> 1) * 2 + tb) * 32 + r32) * 4) = cs;
  }
  BAR();
  float mean1, rstd1;
  {
    f32x2 t = LDS_F2(sb + L_XS + (tb * 128 + r32) * 8);
#pragma unroll
    for (int j = 1; j < 4; ++j) t += LDS_F2(sb + L_XS + (tb * 128 + j * 32 + r32) * 8);
    const float m = t.x * (1.f / 512.f);
    const float var = fmaxf(t.y * (1.f / 512.f) - m * m, 0.f);
    mean1 = rbf(m);
    rstd1 = 1.f / rbf(__builtin_sqrtf(rbf(rbf(var) + 1e-5f)));
  }
  v4u kv0, kv1;
  {
    const unsigned cb0 = sb + L_CS + (w >> 1) * 256;
    float o[16];
    if (hi == 0) {
#pragma unroll
      for (int g = 0; g < 4; ++g) {
        const f32x4 c = LDS_F4(cb0 + (8 * g + 4 * h) * 4) + LDS_F4(cb0 + 128 + (8 * g + 4 * h) * 4);
#pragma unroll
        for (int j = 0; j < 4; ++j) o[4 * g + j] = (acc[4 * g + j] - mean1 * c[j]) * rstd1;
      }
    } else {
      const unsigned tabw = sb + L_TAB + (w & 3) * 256;
      LDS_F1(tabw + h * 128 + r32 * 4) = h ? rstd1 : mean1;
      const float c = LDS_F1(cb0 + r32 * 4) + LDS_F1(cb0 + 128 + r32 * 4);
      asm volatile("s_waitcnt lgkmcnt(0)" ::: "memory");
#pragma unroll
      for (int g = 0; g < 4; ++g) {
        const f32x4 m4 = LDS_F4(tabw + (8 * g + 4 * h) * 4), r4 = LDS_F4(tabw + 128 + (8 * g + 4 * h) * 4);
#pragma unroll
        for (int j = 0; j < 4; ++j) o[4 * g + j] = (acc[4 * g + j] - m4[j] * c) * r4[j];
      }
    }
    kv0 = (v4u){pk2(o[0], o[1]), pk2(o[2], o[3]), pk2(o[4], o[5]), pk2(o[6], o[7])};
    kv1 = (v4u){pk2(o[8], o[9]), pk2(o[10], o[11]), pk2(o[12], o[13]), pk2(o[14], o[15])};
  }
  unsigned g0, g1, l0, l1;
  if (hi == 0) {
    g0 = l0 = (unsigned)(2 * ct * 1024 + lane * 16); g1 = l1 = g0 + 1024;
  } else {
    const int head = 2 * ct + (r32 >> 4), pos = (h * 16 + (r32 & 15)) * 16;
    g0 = 4096 + head * 1024 + pos; g1 = g0 + 512;
    l0 = 4096 + head * 2 * 528 + pos; l1 = l0 + 528;
  }
  const unsigned myblk = (unsigned)(tt * 8 + gg);
  __builtin_amdgcn_raw_buffer_store_b128(kv0, rws, WS_KV + myblk * 16384 + tb * 8192 + g0, 0, AUX_SC1);
  __builtin_amdgcn_raw_buffer_store_b128(kv1, rws, WS_KV + myblk * 16384 + tb * 8192 + g1, 0, AUX_SC1);
  STAMP(2);

#pragma unroll
  for (int i = 0; i < 16; ++i) acc[i] = 0.f;
  cs = 0.f;
  WAIT_VM(8); BAR(); chunk((hi * 4 + ((8 + rot) & 3)) * 128, 3 * 16384, false, false);
  WAIT_VM(6); BAR(); chunk((hi * 4 + ((9 + rot) & 3)) * 128, 4 * 16384, false, false);
  WAIT_VM(4); BAR(); chunk((hi * 4 + ((10 + rot) & 3)) * 128, 0 * 16384, false, false);
  WAIT_VM(2); BAR(); chunk((hi * 4 + ((11 + rot) & 3)) * 128, 1 * 16384, false, false);
  WAIT_VM(0);
  if (lane == 0) __hip_atomic_store((GAS unsigned*)(p.ws + WS_KVFLAG + myblk * 128 + (tb * 4 + (w >> 1)) * 4), tag, RLX_AGENT);
  STAMP(3);
  cs = sum32(cs);
  if (h == 0) LDS_F1(sb + L_CSQ + (ct * 4 + hi * 2 + tb) * 128 + r32 * 4) = cs;
  float keep[8];
  {
    f32x4 s0, s1;
    if (hi == 0) {
      s0 = (f32x4){acc[8], acc[9], acc[10], acc[11]}; s1 = (f32x4){acc[12], acc[13], acc[14], acc[15]};
#pragma unroll
      for (int j = 0; j < 8; ++j) keep[j] = acc[j];
    } else {
      s0 = (f32x4){acc[0], acc[1], acc[2], acc[3]}; s1 = (f32x4){acc[4], acc[5], acc[6], acc[7]};
#pragma unroll
      for (int j = 0; j < 8; ++j) keep[j] = acc[8 + j];
    }
    LDS_F4(sb + L_EX + w * 2048 + lane * 16) = s0;
    LDS_F4(sb + L_EX + w * 2048 + 1024 + lane * 16) = s1;
  }
  BAR();
  v4u qf;
  {
    const f32x4 s0 = LDS_F4(sb + L_EX + (w ^ 4) * 2048 + lane * 16), s1 = LDS_F4(sb + L_EX + (w ^ 4) * 2048 + 1024 + lane * 16);
    const unsigned cq = sb + L_CSQ + ct * 512 + (16 * hi + 4 * h) * 4;
    f32x4 c0 = LDS_F4(cq), c1 = LDS_F4(cq + 32);
#pragma unroll
    for (int j = 1; j < 4; ++j) { c0 += LDS_F4(cq + j * 128); c1 += LDS_F4(cq + j * 128 + 32); }
    const float sc = 0.25f * 1.4426950408889634f;
    float o[8];
#pragma unroll
    for (int j = 0; j < 4; ++j) {
      o[j] = rbf((keep[j] + s0[j] - mean1 * c0[j]) * rstd1) * sc;
      o[4 + j] = rbf((keep[4 + j] + s1[j] - mean1 * c1[j]) * rstd1) * sc;
    }
    qf = (v4u){pk2(o[0], o[1]), pk2(o[2], o[3]), pk2(o[4], o[5]), pk2(o[6], o[7])};
  }
  LDS_U4(sb + (8 + tb) * KVREC + l0) = kv0;
  LDS_U4(sb + (8 + tb) * KVREC + l1) = kv1;
  if (tid < 80) LDS_U4(sb + (tid >> 3) * KVREC + 4096 + (tid & 7) * 528 + 512) = (v4u){ONE2, ONE2, ONE2, ONE2};
  {
    const int st = tt - 4 + (w >> 1);
    if (st >= 0) {
      const unsigned sblk = (unsigned)(st * 8 + gg);
      const GAS unsigned* fl = (const GAS unsigned*)(p.ws + WS_KVFLAG + sblk * 128 + (tb * 4 + (lane & 3)) * 4);
      for (unsigned s = 0; s < SPIN_CAP; ++s) {
        const unsigned f = __hip_atomic_load(fl, RLX_AGENT);
        if (__all(f == tag)) break;
      }
      asm volatile("" ::: "memory");
      STAMP(4);
      v4u t[8];
#pragma unroll
      for (int i = 0; i < 8; ++i)
        t[i] = __builtin_amdgcn_raw_buffer_load_b128(rws, WS_KV + sblk * 16384 + tb * 8192 + i * 1024 + lane * 16, 0, AUX_SC1);
#pragma unroll
      for (int i = 0; i < 4; ++i) LDS_U4(sb + w * KVREC + i * 1024 + lane * 16) = t[i];
#pragma unroll
      for (int i = 0; i < 4; ++i) LDS_U4(sb + w * KVREC + 4096 + (i * 2 + h) * 528 + r32 * 16) = t[4 + i];
    }
  }
  STAMP(5);
  const int th = gg >> 2, cb = gg & 3, cq = w & 3, kh = w >> 2;
  const int r16 = lane & 15, G = lane >> 4;
  unsigned wraw[64];
  v4u xres, wr4;
  {
    const __amdgpu_buffer_rsrc_t rwo = __builtin_amdgcn_make_buffer_rsrc((void*)p.wo, 0, 512 * 1024, 0x00020000);
    const unsigned vo = (unsigned)((kh * 256 + 16 * (G >> 1) + 4 * (G & 1)) * 1024 + (128 * cb + 32 * cq + 2 * r16) * 2);
#pragma unroll
    for (int ks = 0; ks < 8; ++ks)
#pragma unroll
      for (int j = 0; j < 8; ++j)
        wraw[ks * 8 + j] = __builtin_amdgcn_raw_buffer_load_b32(rwo, vo + (j & 3) * 1024, ks * 32768 + (j >> 2) * 8192, 0);
    const int col = 128 * cb + 32 * cq + 8 * G;
    xres = *(const GAS v4u*)(p.x + (size_t)(tt * 64 + th * 32 + kh * 16 + r16) * 1024 + col * 2);
    wr4 = *(const GAS v4u*)(p.wr + col * 2);
  }
  const v4u wrall = *(const GAS v4u*)(p.wr + lane * 16);
  const int head = 2 * ct + hi;
  f32x16 mdiag;
#pragma unroll
  for (int i = 0; i < 16; ++i) mdiag[i] = ((i & 3) + 8 * (i >> 2) + 4 * h <= r32) ? 0.f : -1e30f;
  BAR();
  STAMP(6);
  const int fb = b;
  if (fb < 172) {
    glds16(p.wg + fb * 16, (unsigned)tid * (FF * 2), sb + L_FFW + w * 1024);
    glds16(p.wu + fb * 16, (unsigned)tid * (FF * 2), sb + L_FFW + 8192 + w * 1024);
  } else if (fb < 204) {
#pragma unroll
    for (int i = 0; i < 6; ++i) {
      if (i * 8 + w < 43) glds16(p.wd + (fb - 172) * 32 + (size_t)i * 256 * 1024, (unsigned)((tid >> 1) * 1024 + (tid & 1) * 16), sb + L_FFW + i * 8192 + w * 1024);
    }
  }
  v4u ofrag;
  {
    const unsigned ka = sb + head * 1024 + lane * 16;
    const unsigned va = sb + 4096 + head * 2 * 528 + (r32 < 16 ? (h * 16 + r32) * 16 : 512);
    const int first = 8 - 2 * tt;
    const bool has_far = tb >= first;
    const int k0 = has_far ? tb + 1 : first, k1 = tb + 8;
    f32x16 zero;
#pragma unroll
    for (int i = 0; i < 16; ++i) zero[i] = 0.f;
    float mx;
    {
      const f32x16 s = mfma32(LDS_U4(ka + k1 * KVREC), qf, mdiag);
      mx = s[0];
#pragma unroll
      for (int i = 1; i < 16; ++i) mx = fmaxf(mx, s[i]);
    }
    for (int k = k0; k < k1; ++k) {
      const f32x16 s = mfma32(LDS_U4(ka + k * KVREC), qf, zero);
#pragma unroll
      for (int i = 0; i < 16; ++i) mx = fmaxf(mx, s[i]);
    }
    if (has_far) {
      f32x16 c;
#pragma unroll
      for (int i = 0; i < 16; ++i) c[i] = -1e30f - mdiag[i];
      const f32x16 s = mfma32(LDS_U4(ka + tb * KVREC), qf, c);
#pragma unroll
      for (int i = 0; i < 16; ++i) mx = fmaxf(mx, s[i]);
    }
    mx = fmax32(mx);
    f32x16 o = zero;
    auto tile = [&](int k, const f32x16& c) {
      const f32x16 s = mfma32(LDS_U4(ka + k * KVREC), qf, c);
      const v4u v0 = LDS_U4(va + k * KVREC), v1 = LDS_U4(va + k * KVREC + 528);
      float e[16];
#pragma unroll
      for (int i = 0; i < 16; ++i) e[i] = __builtin_amdgcn_exp2f(s[i]);
      const v4u p0 = (v4u){pk2(e[0], e[1]), pk2(e[2], e[3]), pk2(e[4], e[5]), pk2(e[6], e[7])};
      const v4u p1 = (v4u){pk2(e[8], e[9]), pk2(e[10], e[11]), pk2(e[12], e[13]), pk2(e[14], e[15])};
      o = mfma32(v0, p0, o);
      o = mfma32(v1, p1, o);
    };
    {
      f32x16 c;
#pragma unroll
      for (int i = 0; i < 16; ++i) c[i] = mdiag[i] - mx;
      tile(k1, c);
      if (has_far) {
#pragma unroll
        for (int i = 0; i < 16; ++i) c[i] = (-1e30f - mdiag[i]) - mx;
        tile(tb, c);
      }
#pragma unroll
      for (int i = 0; i < 16; ++i) c[i] = -mx;
      for (int k = k0; k < k1; ++k) tile(k, c);
    }
    const float inv = 1.f / o[8];
    ofrag = (v4u){pk2(o[0] * inv, o[1] * inv), pk2(o[2] * inv, o[3] * inv), pk2(o[4] * inv, o[5] * inv), pk2(o[6] * inv, o[7] * inv)};
  }
  STAMP(7);
  {
    const int ghead = gg * 4 + head;
    const unsigned t16 = (unsigned)((tt * 2 + tb) * 2 + (r32 >> 4));
    const unsigned off = WS_O + (t16 * 16 + (ghead >> 1)) * 1024 + ((r32 & 15) + 16 * ((ghead & 1) * 2 + h)) * 16;
    __builtin_amdgcn_raw_buffer_store_b128(ofrag, rws, off, 0, AUX_SC1);
    WAIT_VM(0);
    if (lane == 0) __hip_atomic_store((GAS unsigned*)(p.ws + WS_OFLAG + myblk * 128 + (tb * 4 + head) * 4), tag, RLX_AGENT);
  }
  STAMP(8);
  v4u wof[8][2];
#pragma unroll
  for (int ks = 0; ks < 8; ++ks)
#pragma unroll
    for (int m = 0; m < 4; ++m) {
      wof[ks][0][m] = __builtin_amdgcn_perm(wraw[ks * 8 + 2 * m + 1], wraw[ks * 8 + 2 * m], 0x05040100u);
      wof[ks][1][m] = __builtin_amdgcn_perm(wraw[ks * 8 + 2 * m + 1], wraw[ks * 8 + 2 * m], 0x07060302u);
    }
  float wrsum;
  {
    float s = 0.f;
#pragma unroll
    for (int j = 0; j < 4; ++j) s = dot2(wrall[j], ONE2, s);
    wrsum = sum32(sum16(fsum_row16(s)));
  }
  {
    const unsigned sblk = (unsigned)(tt * 8 + w);
    const GAS unsigned* fl = (const GAS unsigned*)(p.ws + WS_OFLAG + sblk * 128 + (th * 4 + (lane & 3)) * 4);
    for (unsigned s = 0; s < SPIN_CAP; ++s) {
      const unsigned f = __hip_atomic_load(fl, RLX_AGENT);
      if (__all(f == tag)) break;
    }
    asm volatile("" ::: "memory");
    STAMP(9);
    v4u t[4];
#pragma unroll
    for (int i = 0; i < 4; ++i)
      t[i] = __builtin_amdgcn_raw_buffer_load_b128(rws, WS_O + (((tt * 2 + th) * 2 + (i >> 1)) * 16 + 2 * w + (i & 1)) * 1024 + lane * 16, 0, AUX_SC1);
#pragma unroll
    for (int i = 0; i < 4; ++i) LDS_U4(sb + L_OST + ((i >> 1) * 16 + 2 * w + (i & 1)) * 1024 + lane * 16) = t[i];
  }
  BAR();
  STAMP(10);
  float hv[8];
  {
    f32x4 a[2][2];
#pragma unroll
    for (int i = 0; i < 2; ++i)
#pragma unroll
      for (int j = 0; j < 2; ++j) a[i][j] = (f32x4){0.f, 0.f, 0.f, 0.f};
    const unsigned oa = sb + L_OST + (kh * 16 + kh * 8) * 1024 + lane * 16;
    const unsigned ob = sb + L_OST + ((kh ^ 1) * 16 + kh * 8) * 1024 + lane * 16;
#pragma unroll
    for (int ks = 0; ks < 8; ++ks) {
      const v4u o0 = LDS_U4(oa + ks * 1024), o1 = LDS_U4(ob + ks * 1024);
      a[1][0] = mfma16(wof[ks][0], o1, a[1][0]);
      a[1][1] = mfma16(wof[ks][1], o1, a[1][1]);
      a[0][0] = mfma16(wof[ks][0], o0, a[0][0]);
      a[0][1] = mfma16(wof[ks][1], o0, a[0][1]);
    }
    LDS_F4(sb + w * 2048 + lane * 16) = a[1][0];
    LDS_F4(sb + w * 2048 + 1024 + lane * 16) = a[1][1];
    BAR();
    const f32x4 e0 = a[0][0] + LDS_F4(sb + (w ^ 4) * 2048 + lane * 16);
    const f32x4 e1 = a[0][1] + LDS_F4(sb + (w ^ 4) * 2048 + 1024 + lane * 16);
#pragma unroll
    for (int m = 0; m < 4; ++m) {
      const unsigned ab = pk2(e0[m], e1[m]);
      const unsigned hb = pk2(bflo(xres[m]) + bflo(ab), bfhi(xres[m]) + bfhi(ab));
      hv[2 * m] = bflo(hb); hv[2 * m + 1] = bfhi(hb);
    }
  }
  STAMP(11);
  const unsigned grp = (unsigned)(tt * 2 + th);
  {
    float s1 = 0.f, s2 = 0.f, s3 = 0.f;
#pragma unroll
    for (int m = 0; m < 4; ++m) {
      s1 += hv[2 * m] + hv[2 * m + 1];
      s2 = fmaf(hv[2 * m], hv[2 * m], fmaf(hv[2 * m + 1], hv[2 * m + 1], s2));
      s3 = fmaf(hv[2 * m], bflo(wr4[m]), fmaf(hv[2 * m + 1], bfhi(wr4[m]), s3));
    }
    s1 = sum32(sum16(s1)); s2 = sum32(sum16(s2)); s3 = sum32(sum16(s3));
    if (lane < 16) LDS_F4(sb + L_P3 + (w * 16 + lane) * 16) = (f32x4){s1, s2, s3, 0.f};
    BAR();
    if (cq == 0 && lane < 16) {
      f32x4 t = LDS_F4(sb + L_P3 + (w * 16 + lane) * 16);
#pragma unroll
      for (int j = 1; j < 4; ++j) t += LDS_F4(sb + L_P3 + ((w + j) * 16 + lane) * 16);
      const unsigned off = WS_STATS + (grp * 4 + cb) * 1024 + (kh * 16 + lane) * 32;
      __builtin_amdgcn_raw_buffer_store_b128((v4u){tag, asu(t.x), tag, asu(t.y)}, rws, off, 0, AUX_SC1);
      __builtin_amdgcn_raw_buffer_store_b128((v4u){tag, asu(t.z), tag, 0u}, rws, off + 16, 0, AUX_SC1);
    }
  }
  STAMP(12);
  const int tok = tt * 64 + th * 32 + kh * 16 + r16;
  const int col = 128 * cb + 32 * cq + 8 * G;
  const __amdgpu_buffer_rsrc_t rout = __builtin_amdgcn_make_buffer_rsrc(p.out, 0, 2048 * 1024, 0x00020000);
  unsigned key;
  bool cand;
  v4u outv;
  {
    float s1, s2, s3;
    const unsigned off = WS_STATS + (grp * 4 + G) * 1024 + (kh * 16 + r16) * 32;
    for (unsigned s = 0;; ++s) {
      asm volatile("" ::: "memory");
      const v4u a = __builtin_amdgcn_raw_buffer_load_b128(rws, off, 0, AUX_SC1);
      const v4u c = __builtin_amdgcn_raw_buffer_load_b128(rws, off + 16, 0, AUX_SC1);
      s1 = asf(a.y); s2 = asf(a.w); s3 = asf(c.y);
      if (__all(a.x == tag && a.z == tag && c.x == tag) || s >= SPIN_CAP) break;
    }
    STAMP(13);
    s1 = sum32(sum16(s1)); s2 = sum32(sum16(s2)); s3 = sum32(sum16(s3));
    const float m = s1 * (1.f / 512.f);
    const float var = fmaxf(s2 * (1.f / 512.f) - m * m, 0.f);
    const float mb = rbf(m);
    const float rs = 1.f / rbf(__builtin_sqrtf(rbf(rbf(var) + 1e-5f)));
    const unsigned rb = pk2((s3 - mb * wrsum) * rs, 0.f) & 0xffffu;
    const unsigned ord = (rb & 0x8000u) ? (~rb & 0xffffu) : (rb | 0x8000u);
    key = (ord << 11) | (unsigned)(2047 - tok);
    const unsigned k1 = umax_row16(key);
    const unsigned k2 = umax_row16(key == k1 ? 0u : key);
    cand = key == k1 || key == k2;
    if (cb == 0 && cq == 0 && lane < 8)
      __builtin_amdgcn_raw_buffer_store_b128((v4u){tag, k1, tag, k2}, rws, WS_KEYS + lane * 2048 + (grp * 2 + kh) * 16, 0, AUX_SC1);
    unsigned hn[4], hb[4];
#pragma unroll
    for (int m2 = 0; m2 < 4; ++m2) {
      const unsigned d = pk2(hv[2 * m2] - mb, hv[2 * m2 + 1] - mb);
      hn[m2] = pk2(bflo(d) * rs, bfhi(d) * rs);
      hb[m2] = pk2(hv[2 * m2], hv[2 * m2 + 1]);
      outv[m2] = pk2(hv[2 * m2] + bflo(hn[m2]), hv[2 * m2 + 1] + bfhi(hn[m2]));
    }
    if (cand) {
#pragma unroll
      for (int m2 = 0; m2 < 4; ++m2) {
        const unsigned e0 = __builtin_amdgcn_perm(hb[m2], hn[m2], 0x05040100u), e1 = __builtin_amdgcn_perm(hb[m2], hn[m2], 0x07060302u);
        __builtin_amdgcn_raw_buffer_store_b128((v4u){tag, e0, tag, e1}, rws, WS_HROWS + (unsigned)tok * 4096 + (col + 2 * m2) * 8, 0, AUX_SC1);
      }
    } else {
      __builtin_amdgcn_raw_buffer_store_b128(outv, rout, (unsigned)(tok * 1024 + col * 2), 0, AUX_SC1);
    }
  }
  STAMP(14);
  unsigned K1, K2;
  {
    v4u a, c;
    const unsigned off = WS_KEYS + xcd * 2048 + lane * 16;
    for (unsigned s = 0;; ++s) {
      asm volatile("" ::: "memory");
      a = __builtin_amdgcn_raw_buffer_load_b128(rws, off, 0, AUX_SC1);
      c = __builtin_amdgcn_raw_buffer_load_b128(rws, off + 1024, 0, AUX_SC1);
      if (__all(a.x == tag && a.z == tag && c.x == tag && c.z == tag) || s >= SPIN_CAP) break;
    }
    const unsigned hi1 = max(a.y, c.y);
    const unsigned lo1 = max(min(a.y, c.y), max(a.w, c.w));
    K1 = __builtin_amdgcn_readfirstlane(umax_wave(hi1));
    K2 = __builtin_amdgcn_readfirstlane(umax_wave(hi1 == K1 ? lo1 : hi1));
  }
  STAMP(15);
  if (cand && key != K1 && key != K2)
    __builtin_amdgcn_raw_buffer_store_b128(outv, rout, (unsigned)(tok * 1024 + col * 2), 0, AUX_SC1);
  if (b == 255 && tid == 0) __hip_atomic_store((GAS unsigned*)(p.ws + WS_EPOCH), tag, RLX_AGENT);
  if (fb >= 204) return;
  const unsigned t1 = 2047u - (K1 & 2047u), t2 = 2047u - (K2 & 2047u);
  const unsigned wpriv = sb + L_OST + w * 1024;
  const int q4 = (lane & 15) >> 2, p4 = lane & 3;
  if (fb < 172) {
    v4u g;
    const unsigned off = WS_HROWS + (h ? t2 : t1) * 4096 + (64 * w + 2 * r32) * 8;
    for (unsigned s = 0;; ++s) {
      asm volatile("" ::: "memory");
      g = __builtin_amdgcn_raw_buffer_load_b128(rws, off, 0, AUX_SC1);
      if (__all(g.x == tag && g.z == tag) || s >= SPIN_CAP) break;
    }
    LDS_U1(wpriv + lane * 4) = __builtin_amdgcn_perm(g.w, g.y, 0x05040100u);
    const unsigned wa0 = sb + L_FFW + (p4 >> 1) * 8192 + (64 * w + 8 * G + q4) * 16 + (p4 & 1) * 8;
    const v4u a0 = tr16x2(wa0, 64), a1 = tr16x2(wa0 + 512, 64);
    asm volatile("s_waitcnt lgkmcnt(0)" ::: "memory");
    const v4u b0 = LDS_U4(wpriv + (lane & 1) * 128 + G * 16), b1 = LDS_U4(wpriv + (lane & 1) * 128 + 64 + G * 16);
    f32x4 d = mfma16(a0, b0, (f32x4){0.f, 0.f, 0.f, 0.f});
    d = mfma16(a1, b1, d);
    if (r16 < 2) LDS_F4(sb + L_P3 + 2048 + (w * 2 + r16) * 64 + G * 16) = d;
    BAR();
    STAMP(16);
    if (tid < 16) {
      const int sel = tid >> 3, i = tid & 7;
      float gs = 0.f, us = 0.f;
#pragma unroll
      for (int j = 0; j < 8; ++j) {
        gs += LDS_F1(sb + L_P3 + 2048 + (j * 2 + sel) * 64 + i * 4);
        us += LDS_F1(sb + L_P3 + 2048 + (j * 2 + sel) * 64 + 32 + i * 4);
      }
      gs = rbf(gs); us = rbf(us);
      const float si = rbf(gs / (1.f + __expf(-gs)));
      const unsigned av = pk2(si * us, 0.f) & 0xffffu;
      const unsigned nb = (unsigned)__builtin_amdgcn_update_dpp(0, (int)av, 0xB1, 0xF, 0xF, true);
      if ((tid & 1) == 0)
        __hip_atomic_store((GAS u64*)(p.ws + WS_AGRAN + fb * 64 + (tid >> 1) * 8), (u64)tag | ((u64)(av | (nb << 16)) << 32), RLX_AGENT);
    }
    STAMP(17);
  } else {
    const int ob = fb - 172;
    v2u hg = {0u, 0u};
    if (tid < 32) {
      const GAS u64* hp = (const GAS u64*)(p.ws + WS_HROWS + ((tid >> 4) ? t2 : t1) * 4096 + (16 * ob + (tid & 15)) * 8);
      for (unsigned s = 0; s < SPIN_CAP; ++s) {
        const u64 gq = __hip_atomic_load(hp, RLX_AGENT);
        hg = (v2u){(unsigned)gq, (unsigned)(gq >> 32)};
        if (__all(hg.x == tag)) break;
      }
    }
    const int ksA = w + 8 * G, ksB = w + 8 * (G + 4);
    const bool vB = G < 2 && ksB < 43;
    v4u ga, gb;
    for (unsigned s = 0;; ++s) {
      asm volatile("" ::: "memory");
      ga = __builtin_amdgcn_raw_buffer_load_b128(rws, WS_AGRAN + ksA * 256 + r16 * 16, 0, AUX_SC1);
      gb = __builtin_amdgcn_raw_buffer_load_b128(rws, WS_AGRAN + (vB ? ksB : ksA) * 256 + r16 * 16, 0, AUX_SC1);
      if (__all(ga.x == tag && ga.z == tag && gb.x == tag && gb.z == tag) || s >= SPIN_CAP) break;
    }
    STAMP(16);
    LDS_U2(wpriv + G * 128 + r16 * 8) = (v2u){ga.y, ga.w};
    LDS_U2(wpriv + 512 + G * 128 + r16 * 8) = vB ? (v2u){gb.y, gb.w} : (v2u){0u, 0u};
    asm volatile("s_waitcnt lgkmcnt(0)" ::: "memory");
    f32x4 d = {0.f, 0.f, 0.f, 0.f};
    const unsigned wa0 = sb + L_FFW + (32 * w + 8 * G + q4) * 32 + p4 * 8;
#pragma unroll
    for (int i = 0; i < 6; ++i) {
      if (i * 8 + w < 43) {
        const v4u a = tr16x2(wa0 + i * 8192, 128);
        const v4u bb = LDS_U4(wpriv + i * 128 + G * 32 + (lane & 1) * 16);
        d = mfma16(a, bb, d);
      }
    }
    if (r16 < 2) LDS_F4(sb + L_P3 + 2048 + (w * 2 + r16) * 64 + G * 16) = d;
    BAR();
    if (tid < 32) {
      const int sel = tid >> 4, i = tid & 15;
      float y = 0.f;
#pragma unroll
      for (int j = 0; j < 8; ++j) y += LDS_F1(sb + L_P3 + 2048 + (j * 2 + sel) * 64 + i * 4);
      const unsigned ordk = (sel ? K2 : K1) >> 11;
      const unsigned rb = (ordk & 0x8000u) ? (ordk & 0x7fffu) : (~ordk & 0xffffu);
      const float sg = rbf(1.f / (1.f + __expf(-bflo(rb))));
      y = rbf(rbf(y) * sg);
      const float t = rbf(bflo(hg.y) + y);
      const unsigned ov = pk2(bfhi(hg.y) + t, 0.f) & 0xffffu;
      const unsigned nb = (unsigned)__builtin_amdgcn_update_dpp(0, (int)ov, 0xB1, 0xF, 0xF, true);
      if ((tid & 1) == 0)
        __builtin_amdgcn_raw_buffer_store_b32(ov | (nb << 16), rout, (sel ? t2 : t1) * 1024 + (16 * ob + i) * 2, 0, AUX_SC1);
    }
    STAMP(17);
  }
}
}

extern "C" void kernel_launch(void* const* d_in, const int* in_sizes, int n_in, void* d_out, int out_size, void* d_ws,
                              size_t ws_size, hipStream_t stream) {
  Params p;
  p.x = (const char*)d_in[0]; p.wqkv = (const char*)d_in[1]; p.wo = (const char*)d_in[2];
  p.wr = (const char*)d_in[4]; p.wg = (const char*)d_in[5]; p.wu = (const char*)d_in[6]; p.wd = (const char*)d_in[7];
  p.out = (char*)d_out; p.ws = (char*)d_ws;
  (void)hipFuncSetAttribute((const void*)fused_block_kernel, hipFuncAttributeMaxDynamicSharedMemorySize, L_TOTAL);
  hipLaunchKernelGGL(fused_block_kernel, dim3(256), dim3(512), L_TOTAL, stream, p);
}
```

```cpp
#include <hip/hip_runtime.h>
#include <hip/hip_bf16.h>

typedef unsigned long long u64;
typedef unsigned v4u __attribute__((ext_vector_type(4)));
typedef unsigned v2u __attribute__((ext_vector_type(2)));
typedef float f32x2 __attribute__((ext_vector_type(2)));
typedef float f32x4 __attribute__((ext_vector_type(4)));
typedef float f32x16 __attribute__((ext_vector_type(16)));
typedef __bf16 bf16x2 __attribute__((ext_vector_type(2)));
typedef __bf16 bf16x4 __attribute__((ext_vector_type(4)));
typedef __bf16 bf16x8 __attribute__((ext_vector_type(8)));
#define GAS __attribute__((address_space(1)))
#define LAS __attribute__((address_space(3)))
#define DI __device__ __forceinline__
#define RLX_AGENT __ATOMIC_RELAXED, __HIP_MEMORY_SCOPE_AGENT

#define STAMPS 0

namespace {
constexpr int FF = 1376;
constexpr int AUX_SC1 = 16;
constexpr unsigned SPIN_CAP = 1u << 16;
constexpr unsigned WS_EPOCH = 0;
constexpr unsigned WS_KVFLAG = 4096;
constexpr unsigned WS_OFLAG = WS_KVFLAG + 256 * 128;
constexpr unsigned WS_KEYS = WS_OFLAG + 256 * 128;
constexpr unsigned WS_AGRAN = WS_KEYS + 8 * 2048;
constexpr unsigned WS_STATS = 1u << 18;
constexpr unsigned WS_KV = 1u << 20;
constexpr unsigned WS_O = WS_KV + 256 * 16384;
constexpr unsigned WS_HROWS = WS_O + 128 * 16384;
constexpr unsigned WS_STAMP = WS_HROWS + 2048 * 4096;
constexpr unsigned WS_BYTES = WS_STAMP + 256 * 8 * 32 * 8;
static_assert(WS_AGRAN + 192 * 64 <= WS_STATS && WS_STATS + 128 * 4096 <= WS_KV, "ws map");
constexpr int L_X = 0;
constexpr int L_RING = 65536;
constexpr int L_MISC = L_RING + 5 * 16384;
constexpr int L_TAB = L_MISC;
constexpr int L_XS = L_TAB + 1024;
constexpr int L_CS = L_XS + 1024;
constexpr int L_RED = L_CS + 1536;
constexpr int L_QX = L_MISC + 4096;
constexpr int L_WR = L_QX + 8192;
constexpr int L_KVX = L_RING + 2 * 16384;
constexpr int L_FFW = 0;
constexpr int L_PRIV = 49152;
constexpr int L_TOTAL = 163840;
static_assert(L_WR + 1024 <= L_TOTAL, "lds map");
#define DEPTHV 4
constexpr int DEPTH = DEPTHV;

struct Params {
  const char* x; const char* wqkv; const char* wo; const char* wr; const char* wg; const char* wu; const char* wd;
  char* out; char* ws;
};

DI float asf(unsigned u) { return __builtin_bit_cast(float, u); }
DI unsigned asu(float f) { return __builtin_bit_cast(unsigned, f); }
DI float bflo(unsigned u) { return asf(u << 16); }
DI float bfhi(unsigned u) { return asf(u & 0xffff0000u); }
DI unsigned pk2(float a, float b) { f32x2 f = {a, b}; return __builtin_bit_cast(unsigned, __builtin_convertvector(f, bf16x2)); }
DI float rbf(float a) { return bflo(pk2(a, a)); }
DI float dot2(unsigned a, unsigned b, float c) {
  return __builtin_amdgcn_fdot2_f32_bf16(__builtin_bit_cast(bf16x2, a), __builtin_bit_cast(bf16x2, b), c, false);
}
DI f32x16 mfma32(v4u a, v4u b, f32x16 c) {
  return __builtin_amdgcn_mfma_f32_32x32x16_bf16(__builtin_bit_cast(bf16x8, a), __builtin_bit_cast(bf16x8, b), c, 0, 0, 0);
}
DI f32x4 mfma16(v4u a, v4u b, f32x4 c) {
  return __builtin_amdgcn_mfma_f32_16x16x32_bf16(__builtin_bit_cast(bf16x8, a), __builtin_bit_cast(bf16x8, b), c, 0, 0, 0);
}
DI float sum32(float x) {
  auto r = __builtin_amdgcn_permlane32_swap(asu(x), asu(x), false, false);
  const unsigned r0 = r[0], r1 = r[1];
  return asf(r0) + asf(r1);
}
DI float sum16(float x) {
  auto r = __builtin_amdgcn_permlane16_swap(asu(x), asu(x), false, false);
  const unsigned r0 = r[0], r1 = r[1];
  return asf(r0) + asf(r1);
}
DI float fmax32(float x) {
  auto r = __builtin_amdgcn_permlane32_swap(asu(x), asu(x), false, false);
  const unsigned r0 = r[0], r1 = r[1];
  return fmaxf(asf(r0), asf(r1));
}
template <int C> DI unsigned dppu(unsigned k) { return (unsigned)__builtin_amdgcn_update_dpp(0, (int)k, C, 0xF, 0xF, true); }
template <int C> DI float dppf(float k) { return asf(dppu<C>(asu(k))); }
DI unsigned umax_row16(unsigned k) {
  k = max(k, dppu<0xB1>(k)); k = max(k, dppu<0x4E>(k)); k = max(k, dppu<0x141>(k)); k = max(k, dppu<0x140>(k));
  return k;
}
DI unsigned umax_wave(unsigned k) {
  k = umax_row16(k);
  { auto r = __builtin_amdgcn_permlane16_swap(k, k, false, false); const unsigned r0 = r[0], r1 = r[1]; k = max(r0, r1); }
  { auto r = __builtin_amdgcn_permlane32_swap(k, k, false, false); const unsigned r0 = r[0], r1 = r[1]; k = max(r0, r1); }
  return k;
}
DI float fsum8(float v) { v += dppf<0xB1>(v); v += dppf<0x4E>(v); v += dppf<0x141>(v); return v; }
DI float fsum_row16(float v) { v = fsum8(v); v += dppf<0x140>(v); return v; }
DI void ln_scalars(float s1, float s2, float& mb, float& rs) {
  const float m = s1 * (1.f / 512.f);
  const float var = fmaxf(fmaf(-m, m, s2 * (1.f / 512.f)), 0.f);
  mb = rbf(m);
  rs = __builtin_amdgcn_rcpf(rbf(__builtin_amdgcn_sqrtf(rbf(rbf(var) + 1e-5f))));
}
DI void glds_chunk(unsigned v0, unsigned v1, const char* ba, const char* bb, unsigned m0v) {
  asm volatile("s_mov_b32 m0, %4\n\ts_nop 0\n\tglobal_load_lds_dwordx4 %0, %2\n\t"
               "s_add_u32 m0, m0, 0x1000\n\ts_nop 0\n\tglobal_load_lds_dwordx4 %1, %2\n\t"
               "s_add_u32 m0, m0, 0x1000\n\ts_nop 0\n\tglobal_load_lds_dwordx4 %0, %3\n\t"
               "s_add_u32 m0, m0, 0x1000\n\ts_nop 0\n\tglobal_load_lds_dwordx4 %1, %3"
               :: "v"(v0), "v"(v1), "s"(ba), "s"(bb), "s"(m0v) : "memory", "scc");
}
DI void glds2(unsigned v, const char* ba, const char* bb, unsigned m0v) {
  asm volatile("s_mov_b32 m0, %3\n\ts_nop 0\n\tglobal_load_lds_dwordx4 %0, %1\n\t"
               "s_add_u32 m0, m0, 0x1000\n\ts_nop 0\n\tglobal_load_lds_dwordx4 %0, %2"
               :: "v"(v), "s"(ba), "s"(bb), "s"(m0v) : "memory", "scc");
}
template <int N> DI void wait_vm() { asm volatile("s_waitcnt vmcnt(%0)" :: "n"(N) : "memory"); }
DI void glds1(unsigned v, const char* ba, unsigned m0v) {
  asm volatile("s_mov_b32 m0, %2\n\ts_nop 0\n\tglobal_load_lds_dwordx4 %0, %1" :: "v"(v), "s"(ba), "s"(m0v) : "memory");
}
#define WAIT_VM(n) asm volatile("s_waitcnt vmcnt(" #n ")" ::: "memory")
#define WAIT_LGKM() asm volatile("s_waitcnt lgkmcnt(0)" ::: "memory")
#define BAR() do { __builtin_amdgcn_sched_barrier(0); asm volatile("s_waitcnt lgkmcnt(0)\n\ts_barrier" ::: "memory"); __builtin_amdgcn_sched_barrier(0); } while (0)
#define PIN(x) asm volatile("" : "+v"(x))
#define SPIN_(x) asm volatile("" : "+s"(x))
#define SB() __builtin_amdgcn_sched_barrier(0)
#define LDS_U4(off) (*(LAS v4u*)(size_t)(off))
#define LDS_F4(off) (*(LAS f32x4*)(size_t)(off))
#define LDS_U2(off) (*(LAS v2u*)(size_t)(off))
#define LDS_F1(off) (*(LAS float*)(size_t)(off))
#define LDS_U1(off) (*(LAS unsigned*)(size_t)(off))
DI v2u tr16(unsigned off) { return __builtin_bit_cast(v2u, __builtin_amdgcn_ds_read_tr16_b64_v4bf16((LAS bf16x4*)(size_t)off)); }
DI v4u tr16x2(unsigned off, unsigned d) { const v2u a = tr16(off), b = tr16(off + d); return (v4u){a.x, a.y, b.x, b.y}; }
DI v4u pack8(const float* o) { return (v4u){pk2(o[0], o[1]), pk2(o[2], o[3]), pk2(o[4], o[5]), pk2(o[6], o[7])}; }

#define STAMP(i) do {} while (0)
#define STAMP_OUT() do {} while (0)

constexpr unsigned ONE2 = 0x3F803F80u;
struct Frags { v4u x[4], a[4], b[4]; };
constexpr int npieces(int n) { return n < 8 ? 6 : (n == 12 ? 0 : 4); }
constexpr int issued_before(int j) { return j < 9 ? (j - 1 + DEPTH < 11 ? j - 1 + DEPTH : 11) : j + 3; }
constexpr int younger(int j) { int s = 0; for (int n = j + 1; n <= issued_before(j); ++n) s += npieces(n); return s; }

__global__ __launch_bounds__(512, 2) void fused_block_kernel(Params p) {
  extern __shared__ __attribute__((aligned(16))) char smem[];
  const int lane = threadIdx.x & 63;
  const int wv = __builtin_amdgcn_readfirstlane(threadIdx.x >> 6);
  const int w = wv & 3, tid = threadIdx.x & 255;
  const int b = blockIdx.x, xcd = b & 7, slot = b >> 3;
  const int tt = (xcd & 3) * 8 + (slot & 7), gg = (xcd >> 2) * 4 + (slot >> 3);
  const unsigned sb = (unsigned)(size_t)(LAS char*)smem;
  const int rot = tt & 7;
  const int f1 = slot < 22 ? xcd * 22 + slot : 999;
  const int f2 = (slot >= 22 && slot < 26) ? xcd * 4 + slot - 22 : -1;

  if (wv >= 4) {
    const int wrow = tid >> 3;
    const unsigned xv = (unsigned)(wrow * 1024 + (((tid & 7) ^ ((wrow >> 1) & 7)) << 4));
    const unsigned wcol = (unsigned)(gg * 128 + (((tid & 7) ^ (((wrow >> 1) & 1) << 2)) << 4));
    const unsigned wv0 = (unsigned)wrow * 3072 + wcol, wv1 = wv0 + 32 * 3072;
    const unsigned ov0 = (unsigned)wrow * 1024 + wcol, ov1 = ov0 + 32 * 1024;
    const unsigned ringw = sb + L_RING + w * 1024;
    const char* xb = p.x + (size_t)tt * 65536;
    auto issue = [&](int n) {
      if (n < 8) {
        const int c = (n + rot) & 7;
        glds2(xv, xb + c * 128, xb + c * 128 + 32768, sb + L_X + n * 8192 + w * 1024);
        const char* s0 = p.wqkv + (size_t)(c * 64) * 3072 + 1024;
        glds_chunk(wv0, wv1, s0, s0 + 1024, ringw + (n % 5) * 16384);
      } else if (n < 12) {
        glds_chunk(wv0, wv1, p.wqkv + (size_t)(((2 * n + rot) & 7) * 64) * 3072, p.wqkv + (size_t)(((2 * n + 1 + rot) & 7) * 64) * 3072,
                   ringw + (n % 5) * 16384);
      } else if (n > 12) {
        const char* s0 = p.wo + (size_t)(((n - 13 + rot) & 3) * 128) * 1024;
        glds_chunk(ov0, ov1, s0, s0 + 64 * 1024, ringw + (n % 5) * 16384);
      }
    };
#pragma unroll
    for (int n = 0; n < DEPTH; ++n) issue(n);
#define LSTEP(j) do { wait_vm<younger(j)>(); asm volatile("s_barrier" ::: "memory"); \
      if ((j) < 9) { if ((j) + DEPTH < 12) issue((j) + DEPTH); } else issue((j) + 4); } while (0)
    LSTEP(0); LSTEP(1); LSTEP(2); LSTEP(3); LSTEP(4); LSTEP(5); LSTEP(6); LSTEP(7); LSTEP(8); LSTEP(9); LSTEP(10); LSTEP(11);
    asm volatile("s_barrier" ::: "memory");
    issue(16);
    if (w == 0) glds1((unsigned)lane * 16, p.wr, sb + L_WR);
    asm volatile("s_barrier" ::: "memory");
    WAIT_VM(0);
    asm volatile("s_barrier" ::: "memory");
    if (f1 < 172) {
#pragma unroll
      for (int i = 0; i < 2; ++i) {
        glds1((unsigned)tid * (FF * 2), p.wg + f1 * 16 + (size_t)i * 256 * FF * 2, sb + L_FFW + i * 4096 + w * 1024);
        glds1((unsigned)tid * (FF * 2), p.wu + f1 * 16 + (size_t)i * 256 * FF * 2, sb + L_FFW + 8192 + i * 4096 + w * 1024);
      }
    } else if (f2 >= 0) {
#pragma unroll 1
      for (int i = w; i < 43; i += 4)
        glds1((unsigned)((lane >> 1) * 1024 + (lane & 1) * 16), p.wd + f2 * 32 + (size_t)i * 32 * 1024, sb + L_FFW + i * 1024);
    }
    STAMP(1);
    WAIT_VM(0);
    STAMP(2);
    STAMP_OUT();
    if (f1 < 172 || f2 >= 0) asm volatile("s_barrier" ::: "memory");
    return;
  }

  const unsigned tag = *(const GAS unsigned*)(p.ws + WS_EPOCH) + 1u;
  const int r32 = lane & 31, h = lane >> 5, r16 = lane & 15, G = lane >> 4, q4 = r16 >> 2, p4 = lane & 3;
  STAMP(0);
  const __amdgpu_buffer_rsrc_t rws = __builtin_amdgcn_make_buffer_rsrc(p.ws, 0, WS_BYTES, 0x00020000);
  const int tb = w & 1, ct = w >> 1;
  const unsigned myblk = (unsigned)(tt * 8 + gg);
  unsigned so_kv = WS_KV + myblk * 16384, so_kvf = WS_KVFLAG + myblk * 128 + w * 4;
  unsigned so_ow = WS_O + (tt * 64 + gg * 2 + (w >> 1)) * 1024, so_of = WS_OFLAG + tt * 128;
  unsigned so_or = WS_O + (tt * 4 + w) * 16384, so_st = WS_STATS + (tt * 4 + w) * 2048;
  unsigned so_key = WS_KEYS + (tt * 4 + w) * 16, so_keyr = WS_KEYS + xcd * 2048;
  SPIN_(so_kv); SPIN_(so_kvf); SPIN_(so_ow); SPIN_(so_of); SPIN_(so_or); SPIN_(so_st); SPIN_(so_key); SPIN_(so_keyr);
  unsigned xa[4], wa[4], wb[4];
#pragma unroll
  for (int i = 0; i < 4; ++i) {
    const int ks = i ^ (ct | (tb << 1));
    xa[i] = sb + L_X + (tb * 32 + r32) * 128 + (((2 * ks + h) ^ ((r32 >> 1) & 7)) << 4);
    wa[i] = sb + L_RING + (16 * ks + 8 * h + q4) * 128 + ((ct ^ ((q4 >> 1) & 1)) << 6) + (G & 1) * 32 + p4 * 8;
    wb[i] = wa[i] + 49152;
    PIN(xa[i]); PIN(wa[i]); PIN(wb[i]);
  }
  unsigned kvfo;
  {
    int st = tt - 4 + ((lane & 7) >> 1);
    st = st < 0 ? tt - 1 : st;
    kvfo = WS_KVFLAG + (st * 8 + gg) * 128 + ((w >> 1) * 2 + (lane & 1)) * 4;
    PIN(kvfo);
  }
  f32x16 acc0, acc1;
#pragma unroll
  for (int i = 0; i < 16; ++i) { acc0[i] = 0.f; acc1[i] = 0.f; }
  float cs0 = 0.f, cs1 = 0.f, xs = 0.f, xq = 0.f;
  auto rd = [&](Frags& f, int slab, int sl) {
#pragma unroll
    for (int i = 0; i < 4; ++i) {
      const unsigned wo = sl < 3 ? wa[i] + sl * 16384 : wb[i] + (sl - 3) * 16384;
      f.x[i] = LDS_U4(xa[i] + slab * 8192); f.a[i] = tr16x2(wo, 512); f.b[i] = tr16x2(wo + 8192, 512);
    }
  };
  auto sums = [&](const Frags& f, bool withx) {
    PIN(acc0);
    if (withx) PIN(acc1);
#pragma unroll
    for (int i = 0; i < 8; ++i) { __builtin_amdgcn_sched_group_barrier(0x8, 1, 0); __builtin_amdgcn_sched_group_barrier(0x100, 3, 0); }
    SB();
#pragma unroll
    for (int j = 0; j < 4; ++j) {
      cs0 = dot2(f.a[0][j], ONE2, cs0); cs1 = dot2(f.b[0][j], ONE2, cs1);
      if (withx) { xs = dot2(f.x[0][j], ONE2, xs); xq = dot2(f.x[0][j], f.x[0][j], xq); }
    }
#pragma unroll
    for (int j = 0; j < 4; ++j) {
      cs0 = dot2(f.a[1][j], ONE2, cs0); cs1 = dot2(f.b[1][j], ONE2, cs1);
      if (withx) { xs = dot2(f.x[2][j], ONE2, xs); xq = dot2(f.x[2][j], f.x[2][j], xq); }
    }
    PIN(cs0); PIN(cs1);
    if (withx) { PIN(xs); PIN(xq); }
  };
  auto mmA = [&](const Frags& f) {
#pragma unroll
    for (int i = 0; i < 4; ++i) { acc0 = mfma32(f.a[i], f.x[i], acc0); acc1 = mfma32(f.x[i], f.b[i], acc1); }
  };
  Frags fa, fb2;
  BAR(); rd(fa, 0, 0); sums(fa, true);
  STAMP(20);
  BAR(); rd(fb2, 1, 1); mmA(fa); sums(fb2, true);
  STAMP(21);
  BAR(); rd(fa, 2, 2); mmA(fb2); sums(fa, true);
  STAMP(22);
  BAR(); rd(fb2, 3, 3); mmA(fa); sums(fb2, true);
  STAMP(23);
  BAR(); rd(fa, 4, 4); mmA(fb2); sums(fa, true);
  STAMP(24);
  BAR(); rd(fb2, 5, 0); mmA(fa); sums(fb2, true);
  STAMP(25);
  BAR(); rd(fa, 6, 1); mmA(fb2); sums(fa, true);
  STAMP(26);
  BAR(); rd(fb2, 7, 2); mmA(fa); sums(fb2, true);
  STAMP(27);
  cs0 = sum32(cs0); cs1 = sum32(cs1); xs = sum32(xs); xq = sum32(xq);
  LDS_F1(sb + L_CS + h * 512 + w * 128 + r32 * 4) = h ? cs1 : cs0;
  LDS_F1(sb + L_XS + w * 256 + r32 * 8 + h * 4) = h ? xq : xs;
  cs0 = 0.f; cs1 = 0.f;
  BAR();
  float rs1, mr1;
  {
    const v2u t0 = LDS_U2(sb + L_XS + tb * 256 + r32 * 8), t1 = LDS_U2(sb + L_XS + 512 + tb * 256 + r32 * 8);
    SB();
    mmA(fb2);
    float mb;
    ln_scalars(asf(t0.x) + asf(t1.x), asf(t0.y) + asf(t1.y), mb, rs1);
    mr1 = -mb * rs1;
    LDS_F1(sb + L_TAB + w * 256 + lane * 4) = h ? mr1 : rs1;
  }
  STAMP(1);
  v4u xh[4];
  auto rdx = [&](int slab) {
#pragma unroll
    for (int i = 0; i < 4; ++i) xh[i] = LDS_U4(xa[i] + slab * 8192);
  };
  {
    float o[16];
    const unsigned ck = sb + L_CS + ct * 256 + h * 16;
#pragma unroll
    for (int g = 0; g < 4; ++g) {
      const f32x4 c = LDS_F4(ck + g * 32) + LDS_F4(ck + 128 + g * 32);
#pragma unroll
      for (int j = 0; j < 4; ++j) o[4 * g + j] = fmaf(acc0[4 * g + j], rs1, mr1 * c[j]);
    }
    const v4u k0 = pack8(o), k1 = pack8(o + 8);
    const unsigned ko = tb * 8192 + 2 * ct * 1024 + lane * 16;
    __builtin_amdgcn_raw_buffer_store_b128(k0, rws, ko, so_kv, AUX_SC1);
    __builtin_amdgcn_raw_buffer_store_b128(k1, rws, ko + 1024, so_kv, AUX_SC1);
    const float cv = LDS_F1(sb + L_CS + 512 + ct * 256 + r32 * 4) + LDS_F1(sb + L_CS + 512 + ct * 256 + 128 + r32 * 4);
    const unsigned tv = sb + L_TAB + w * 256 + h * 16;
#pragma unroll
    for (int g = 0; g < 4; ++g) {
      const f32x4 r4 = LDS_F4(tv + g * 32), m4 = LDS_F4(tv + 128 + g * 32);
#pragma unroll
      for (int j = 0; j < 4; ++j) o[4 * g + j] = fmaf(acc1[4 * g + j], r4[j], m4[j] * cv);
    }
    const v4u v0 = pack8(o), v1 = pack8(o + 8);
    const unsigned vo = tb * 8192 + 4096 + (2 * ct + (r32 >> 4)) * 1024 + (h * 32 + r16) * 16;
    __builtin_amdgcn_raw_buffer_store_b128(v0, rws, vo, so_kv, AUX_SC1);
    __builtin_amdgcn_raw_buffer_store_b128(v1, rws, vo + 256, so_kv, AUX_SC1);
    SB();
    rd(fa, 0, 3); rdx(1);
    LDS_U4(sb + L_KVX + ko) = k0; LDS_U4(sb + L_KVX + ko + 1024) = k1;
    LDS_U4(sb + L_KVX + vo) = v0; LDS_U4(sb + L_KVX + vo + 256) = v1;
  }
  STAMP(2);
#pragma unroll
  for (int i = 0; i < 16; ++i) acc0[i] = 0.f;
  auto mmB = [&](const Frags& f) {
#pragma unroll
    for (int i = 0; i < 4; ++i) acc0 = mfma32(f.b[i], xh[i], acc0);
#pragma unroll
    for (int i = 0; i < 4; ++i) acc0 = mfma32(f.a[i], f.x[i], acc0);
  };
  sums(fa, false);
  BAR(); rd(fb2, 2, 4); mmB(fa); rdx(3); sums(fb2, false);
  BAR(); rd(fa, 4, 0); mmB(fb2); rdx(5); sums(fa, false);
  BAR(); rd(fb2, 6, 1); mmB(fa); rdx(7); sums(fb2, false);
  v4u xres0, xres1;
  {
    const int row = w * 16 + r16;
    const unsigned xr = sb + L_X + ((gg - rot) & 7) * 8192 + row * 128;
    xres0 = LDS_U4(xr + ((G ^ ((row >> 1) & 7)) << 4)); xres1 = LDS_U4(xr + (((G + 4) ^ ((row >> 1) & 7)) << 4));
    PIN(xres0); PIN(xres1);
  }
  cs0 = sum32(cs0 + cs1);
  if (h == 0) LDS_F1(sb + L_CS + 1024 + w * 128 + r32 * 4) = cs0;
  WAIT_VM(0);
  if (lane == 0) __builtin_amdgcn_raw_buffer_store_b32(tag, rws, 0, so_kvf, AUX_SC1);
  BAR();
  mmB(fb2);
  STAMP(3);
  {
    float o[16];
    const unsigned ck = sb + L_CS + 1024 + ct * 256 + h * 16;
    const float sc = 0.25f * 1.4426950408889634f;
#pragma unroll
    for (int g = 0; g < 4; ++g) {
      const f32x4 c = LDS_F4(ck + g * 32) + LDS_F4(ck + 128 + g * 32);
#pragma unroll
      for (int j = 0; j < 4; ++j) o[4 * g + j] = fmaf(acc0[4 * g + j], rs1, mr1 * c[j]);
    }
    const v4u a0 = pack8(o), a1 = pack8(o + 8);
#pragma unroll
    for (int j = 0; j < 4; ++j) { o[2 * j] = bflo(a0[j]) * sc; o[2 * j + 1] = bfhi(a0[j]) * sc; o[8 + 2 * j] = bflo(a1[j]) * sc; o[9 + 2 * j] = bfhi(a1[j]) * sc; }
    LDS_U4(sb + L_QX + (tb * 4 + 2 * ct) * 1024 + lane * 16) = pack8(o);
    LDS_U4(sb + L_QX + (tb * 4 + 2 * ct + 1) * 1024 + lane * 16) = pack8(o + 8);
  }
  BAR();
  {
    v4u kf[10], va[10], vb[10], qf[2];
    const unsigned lo = w * 1024 + lane * 16;
    qf[0] = LDS_U4(sb + L_QX + lo); qf[1] = LDS_U4(sb + L_QX + 4096 + lo);
    kf[8] = LDS_U4(sb + L_KVX + lo); kf[9] = LDS_U4(sb + L_KVX + 8192 + lo);
    va[8] = LDS_U4(sb + L_KVX + 4096 + lo); va[9] = LDS_U4(sb + L_KVX + 12288 + lo);
    auto vfix = [&](int k) {
#pragma unroll
      for (int j = 0; j < 4; ++j) {
        auto r = __builtin_amdgcn_permlane16_swap(va[k][j], ONE2, false, false);
        const unsigned r0 = r[0], r1 = r[1];
        va[k][j] = r0; vb[k][j] = r1;
      }
    };
    f32x16 zero;
#pragma unroll
    for (int i = 0; i < 16; ++i) zero[i] = 0.f;
    bool vis[16];
#pragma unroll
    for (int i = 0; i < 16; ++i) vis[i] = (i & 3) + 8 * (i >> 2) + 4 * h <= r32;
    unsigned pm[8];
#pragma unroll
    for (int i = 0; i < 8; ++i) { pm[i] = (vis[2 * i] ? 0xffffu : 0u) | (vis[2 * i + 1] ? 0xffff0000u : 0u); PIN(pm[i]); }
    float ma[2], mb[2];
    auto mx16 = [&](int qb, const f32x16& s) {
#pragma unroll
      for (int i = 0; i < 16; i += 4) { ma[qb] = fmaxf(fmaxf(ma[qb], s[i]), s[i + 1]); mb[qb] = fmaxf(fmaxf(mb[qb], s[i + 2]), s[i + 3]); }
      PIN(ma[qb]); PIN(mb[qb]);
    };
    ma[0] = mb[0] = ma[1] = mb[1] = -3e38f;
    f32x16 sm[2];
    sm[0] = mfma32(kf[8], qf[0], zero); sm[1] = mfma32(kf[9], qf[1], zero);
    mx16(1, mfma32(kf[8], qf[1], zero));
    vfix(8); vfix(9);
    PIN(sm[0]); PIN(sm[1]); PIN(ma[1]); PIN(mb[1]);
    PIN(va[8]); PIN(vb[8]); PIN(va[9]); PIN(vb[9]);
    unsigned ooff0 = ((unsigned)(r32 >> 4) * 16) * 1024 + (r16 + 16 * ((w & 1) * 2 + h)) * 16, ooff1 = ooff0 + 32768;
    PIN(ooff0); PIN(ooff1);
    auto ostore = [&](int qb, const f32x16& o) {
      const float inv = __builtin_amdgcn_rcpf(o[8]);
      float on[8];
#pragma unroll
      for (int i = 0; i < 8; ++i) on[i] = o[i] * inv;
      __builtin_amdgcn_raw_buffer_store_b128(pack8(on), rws, qb ? ooff1 : ooff0, so_ow, AUX_SC1);
    };
    auto pv = [&](const f32x16& s, int k, f32x16& o) {
      float e[16];
#pragma unroll
      for (int i = 0; i < 16; ++i) e[i] = __builtin_amdgcn_exp2f(s[i]);
      o = mfma32(va[k], pack8(e), o);
      o = mfma32(vb[k], pack8(e + 8), o);
    };
    if (tt > 0) {
      for (unsigned s = 0; s < SPIN_CAP; ++s) {
        asm volatile("" ::: "memory");
        const unsigned f = __builtin_amdgcn_raw_buffer_load_b32(rws, kvfo, 0, AUX_SC1);
        if (__all(f == tag)) break;
      }
      asm volatile("" ::: "memory");
    }
    STAMP(4);
    const unsigned so = so_kv - 32 * 16384;
    if (tt >= 4) {
#pragma unroll
      for (int k = 0; k < 8; ++k) kf[k] = __builtin_amdgcn_raw_buffer_load_b128(rws, lo, so + (k >> 1) * 131072 + (k & 1) * 8192, AUX_SC1);
#pragma unroll
      for (int k = 0; k < 8; ++k) va[k] = __builtin_amdgcn_raw_buffer_load_b128(rws, lo, so + (k >> 1) * 131072 + (k & 1) * 8192 + 4096, AUX_SC1);
      SB();
      STAMP(5);
      f32x16 s0 = mfma32(kf[0], qf[0], zero), s1 = mfma32(kf[1], qf[1], zero), s2 = mfma32(kf[1], qf[0], zero);
      SB();
#pragma unroll
      for (int i = 0; i < 16; ++i) sm[0][i] = vis[i] ? sm[0][i] : s0[i];
      s0 = mfma32(kf[2], qf[0], zero);
      SB();
#pragma unroll
      for (int i = 0; i < 16; ++i) sm[1][i] = vis[i] ? sm[1][i] : s1[i];
      s1 = mfma32(kf[2], qf[1], zero);
      SB();
      mx16(0, s2); mx16(0, sm[0]); mx16(1, sm[1]);
      SB();
#pragma unroll
      for (int k = 3; k < 8; ++k) {
        s2 = mfma32(kf[k], qf[0], zero); SB();
        mx16(0, s0); SB();
        s0 = mfma32(kf[k], qf[1], zero); SB();
        mx16(1, s1); SB();
        s1 = s0; s0 = s2;
      }
      vfix(0); vfix(1);
      SB();
      asm volatile("s_barrier" ::: "memory");
      STAMP(6);
      mx16(0, s0); mx16(1, s1);
      const float m0 = fmax32(fmaxf(ma[0], mb[0])), m1 = fmax32(fmaxf(ma[1], mb[1]));
#pragma unroll
      for (int qb = 0; qb < 2; ++qb) {
        const float mx = qb ? m1 : m0;
        f32x16 c, o;
        v4u pp;
#pragma unroll
        for (int i = 0; i < 16; ++i) c[i] = -mx;
        SB();
        s0 = mfma32(kf[1 + qb], qf[qb], c);
        PIN(s0);
        SB();
        {
          float e[16];
#pragma unroll
          for (int i = 0; i < 16; ++i) e[i] = __builtin_amdgcn_exp2f(sm[qb][i] - mx);
          const v4u p0 = pack8(e), p1 = pack8(e + 8);
          v4u d0, d1, f0, f1;
#pragma unroll
          for (int j = 0; j < 4; ++j) { d0[j] = p0[j] & pm[j]; f0[j] = p0[j] & ~pm[j]; d1[j] = p1[j] & pm[4 + j]; f1[j] = p1[j] & ~pm[4 + j]; }
          o = mfma32(va[8 + qb], d0, zero);
          o = mfma32(va[qb], f0, o);
          o = mfma32(vb[8 + qb], d1, o);
          pp = f1;
        }
        SB();
#pragma unroll
        for (int k = 1 + qb; k < 8 + qb; ++k) {
          float e[16];
#pragma unroll
          for (int i = 0; i < 4; ++i) e[i] = __builtin_amdgcn_exp2f(s0[i]);
          SB();
          o = mfma32(vb[k - 1], pp, o);
          PIN(o);
          SB();
#pragma unroll
          for (int i = 4; i < 8; ++i) e[i] = __builtin_amdgcn_exp2f(s0[i]);
          SB();
          if (k + 1 < 8 + qb) { s1 = mfma32(kf[k + 1], qf[qb], c); PIN(s1); }
          SB();
          const v4u p0 = pack8(e);
          e[8] = __builtin_amdgcn_exp2f(s0[8]); e[9] = __builtin_amdgcn_exp2f(s0[9]);
          SB();
          o = mfma32(va[k], p0, o);
          PIN(o);
          SB();
#pragma unroll
          for (int i = 10; i < 16; ++i) e[i] = __builtin_amdgcn_exp2f(s0[i]);
          pp = pack8(e + 8);
          if (qb == 0 && k + 1 < 8) vfix(k + 1);
          SB();
          s0 = s1;
        }
        o = mfma32(vb[7 + qb], pp, o);
        ostore(qb, o);
      }
    } else {
#pragma unroll
      for (int t = 1; t < 4; ++t) {
        if (tt - 4 + t >= 0) {
#pragma unroll
          for (int k = 2 * t; k < 2 * t + 2; ++k) {
            kf[k] = __builtin_amdgcn_raw_buffer_load_b128(rws, lo, so + (k >> 1) * 131072 + (k & 1) * 8192, AUX_SC1);
            va[k] = __builtin_amdgcn_raw_buffer_load_b128(rws, lo, so + (k >> 1) * 131072 + (k & 1) * 8192 + 4096, AUX_SC1);
          }
#pragma unroll
          for (int k = 2 * t; k < 2 * t + 2; ++k) { mx16(0, mfma32(kf[k], qf[0], zero)); mx16(1, mfma32(kf[k], qf[1], zero)); vfix(k); }
        }
      }
      asm volatile("s_barrier" ::: "memory");
#pragma unroll
      for (int qb = 0; qb < 2; ++qb) {
#pragma unroll
        for (int i = 0; i < 16; ++i) sm[qb][i] = vis[i] ? sm[qb][i] : -1e30f;
        mx16(qb, sm[qb]);
        const float mx = fmax32(fmaxf(ma[qb], mb[qb]));
        f32x16 c, o = zero;
#pragma unroll
        for (int i = 0; i < 16; ++i) { c[i] = -mx; sm[qb][i] -= mx; }
        pv(sm[qb], 8 + qb, o);
        if (qb == 1) pv(mfma32(kf[8], qf[1], c), 8, o);
#pragma unroll
        for (int t = 1; t < 4; ++t) {
          if (tt - 4 + t >= 0) { pv(mfma32(kf[2 * t], qf[qb], c), 2 * t, o); pv(mfma32(kf[2 * t + 1], qf[qb], c), 2 * t + 1, o); }
        }
        ostore(qb, o);
      }
    }
  }
  STAMP(7);
  WAIT_VM(0);
  if (lane == 0) __builtin_amdgcn_raw_buffer_store_b32(tag, rws, (gg * 4 + w) * 4, so_of, AUX_SC1);
  const int tok = tt * 64 + w * 16 + r16;
  const int col = 64 * gg + 8 * G;
  STAMP(8);
  STAMP(10);
  const unsigned oa = sb + L_RING + (16 * (G >> 1) + 4 * (G & 1) + q4) * 128 + p4 * 16;
  const unsigned oe = oa + ((q4 >> 1) & 1) * 64, oo = oa + (((q4 >> 1) & 1) ^ 1) * 64;
  auto wofrag = [&](int qn, int ks, int c) {
    const unsigned base = ((c >> 1) ? oo : oe) + (qn == 0 ? 3 : qn == 1 ? 4 : qn - 2) * 16384 + ks * 4096 + (c & 1) * 8;
    return tr16x2(base, 1024);
  };
  v4u wf[2][4][4];
#pragma unroll
  for (int qn = 0; qn < 2; ++qn)
#pragma unroll
    for (int ks = 0; ks < 4; ++ks)
#pragma unroll
      for (int c = 0; c < 4; ++c) wf[qn][ks][c] = wofrag(qn, ks, c);
  v4u wr0 = LDS_U4(sb + L_WR + col * 2), wr1 = LDS_U4(sb + L_WR + col * 2 + 64);
  const v4u wrall = LDS_U4(sb + L_WR + lane * 16);
  float wrsum;
  {
    float s = 0.f;
#pragma unroll
    for (int j = 0; j < 4; ++j) s = dot2(wrall[j], ONE2, s);
    wrsum = sum32(sum16(fsum_row16(s)));
    PIN(wrsum);
  }
#pragma unroll
  for (int qn = 0; qn < 2; ++qn)
#pragma unroll
    for (int ks = 0; ks < 4; ++ks)
#pragma unroll
      for (int c = 0; c < 4; ++c) PIN(wf[qn][ks][c]);
  PIN(wr0); PIN(wr1);
  float hv[16];
  {
    unsigned oq[4];
#pragma unroll
    for (int qn = 0; qn < 4; ++qn) { oq[qn] = so_or + ((qn + rot) & 3) * 4096; SPIN_(oq[qn]); }
    for (unsigned s = 0; s < SPIN_CAP; ++s) {
      asm volatile("" ::: "memory");
      const unsigned f = __builtin_amdgcn_raw_buffer_load_b32(rws, r32 * 4, so_of, AUX_SC1);
      if (__all(f == tag)) break;
    }
    asm volatile("" ::: "memory");
    STAMP(9);
    v4u of[4][4];
#pragma unroll
    for (int qn = 0; qn < 4; ++qn)
#pragma unroll
      for (int ks = 0; ks < 4; ++ks) of[qn][ks] = __builtin_amdgcn_raw_buffer_load_b128(rws, lane * 16 + ks * 1024, oq[qn], AUX_SC1);
    asm volatile("" ::: "memory");
    SB();
    f32x4 a[4];
#pragma unroll
    for (int c = 0; c < 4; ++c) a[c] = (f32x4){0.f, 0.f, 0.f, 0.f};
#pragma unroll
    for (int qn = 0; qn < 2; ++qn)
#pragma unroll
      for (int ks = 0; ks < 4; ++ks)
#pragma unroll
        for (int c = 0; c < 4; ++c) {
          a[c] = mfma16(wf[qn][ks][c], of[qn][ks], a[c]);
          wf[qn][ks][c] = wofrag(qn + 2, ks, c);
        }
#pragma unroll
    for (int c = 0; c < 4; ++c) PIN(a[c]);
#pragma unroll
    for (int i = 0; i < 32; ++i) { __builtin_amdgcn_sched_group_barrier(0x8, 1, 0); __builtin_amdgcn_sched_group_barrier(0x100, 2, 0); }
    SB();
#pragma unroll
    for (int qn = 0; qn < 2; ++qn)
#pragma unroll
      for (int ks = 0; ks < 4; ++ks)
#pragma unroll
        for (int c = 0; c < 4; ++c) a[c] = mfma16(wf[qn][ks][c], of[qn + 2][ks], a[c]);
#pragma unroll
    for (int c = 0; c < 4; ++c)
#pragma unroll
      for (int m = 0; m < 2; ++m) {
        const unsigned xr = (c < 2 ? xres0 : xres1)[(c & 1) * 2 + m];
        const unsigned ab = pk2(a[c][2 * m], a[c][2 * m + 1]);
        const unsigned hb = pk2(bflo(xr) + bflo(ab), bfhi(xr) + bfhi(ab));
        hv[4 * c + 2 * m] = bflo(hb); hv[4 * c + 2 * m + 1] = bfhi(hb);
      }
  }
  STAMP(11);
  {
    float s1 = 0.f, s2 = 0.f, s3 = 0.f;
#pragma unroll
    for (int m = 0; m < 8; ++m) {
      const unsigned wv = (m < 4 ? wr0 : wr1)[m & 3];
      s1 += hv[2 * m] + hv[2 * m + 1];
      s2 = fmaf(hv[2 * m], hv[2 * m], fmaf(hv[2 * m + 1], hv[2 * m + 1], s2));
      s3 = fmaf(hv[2 * m], bflo(wv), fmaf(hv[2 * m + 1], bfhi(wv), s3));
    }
    s1 = sum32(sum16(s1)); s2 = sum32(sum16(s2)); s3 = sum32(sum16(s3));
    if (lane < 16)
      __builtin_amdgcn_raw_buffer_store_b128((v4u){asu(s1), asu(s2), asu(s3), tag ^ asu(s1) ^ asu(s2) ^ asu(s3)}, rws, gg * 256 + r16 * 16, so_st, AUX_SC1);
  }
  STAMP(12);
  const __amdgpu_buffer_rsrc_t rout = __builtin_amdgcn_make_buffer_rsrc(p.out, 0, 2048 * 1024, 0x00020000);
  const unsigned wpriv = sb + L_PRIV + w * 2048;
  unsigned key;
  v4u out0, out1;
  {
    float s1, s2, s3;
    for (unsigned s = 0;; ++s) {
      asm volatile("" ::: "memory");
      const v4u a = __builtin_amdgcn_raw_buffer_load_b128(rws, lane * 16, so_st, AUX_SC1);
      const v4u c = __builtin_amdgcn_raw_buffer_load_b128(rws, lane * 16 + 1024, so_st, AUX_SC1);
      s1 = asf(a.x) + asf(c.x); s2 = asf(a.y) + asf(c.y); s3 = asf(a.z) + asf(c.z);
      if (__all((a.x ^ a.y ^ a.z ^ a.w) == tag && (c.x ^ c.y ^ c.z ^ c.w) == tag) || s >= SPIN_CAP) break;
      __builtin_amdgcn_s_sleep(1);
    }
    STAMP(13);
    s1 = sum32(sum16(s1)); s2 = sum32(sum16(s2)); s3 = sum32(sum16(s3));
    float mb, rs;
    ln_scalars(s1, s2, mb, rs);
    const unsigned rb = pk2((s3 - mb * wrsum) * rs, 0.f) & 0xffffu;
    const unsigned ord = (rb & 0x8000u) ? (~rb & 0xffffu) : (rb | 0x8000u);
    key = (ord << 11) | (unsigned)(2047 - tok);
    const unsigned k1 = umax_row16(key);
    const unsigned k2 = umax_row16(key == k1 ? 0u : key);
    if (gg == 0 && lane < 8)
      __builtin_amdgcn_raw_buffer_store_b128((v4u){tag, k1, tag, k2}, rws, lane * 2048, so_key, AUX_SC1);
    SB();
    unsigned hn[8];
#pragma unroll
    for (int m = 0; m < 8; ++m) {
      const unsigned d = pk2(hv[2 * m] - mb, hv[2 * m + 1] - mb);
      hn[m] = pk2(bflo(d) * rs, bfhi(d) * rs);
    }
    if (key == k1 || key == k2) {
      const unsigned base = wpriv + (key == k1 ? 0 : 256) + G * 32;
#pragma unroll
      for (int q = 0; q < 4; ++q) {
        const unsigned a0 = hn[2 * q], a1 = hn[2 * q + 1];
        LDS_U4(base + (q & 1) * 16 + (q >> 1) * 128) = (v4u){(a0 & 0xffffu) | asu(hv[4 * q]), (a0 >> 16) | asu(hv[4 * q + 1]),
                                                              (a1 & 0xffffu) | asu(hv[4 * q + 2]), (a1 >> 16) | asu(hv[4 * q + 3])};
      }
    }
    WAIT_LGKM();
    {
      const v2u e = LDS_U2(wpriv + lane * 8);
      const unsigned trow = 2047u - ((h ? k2 : k1) & 2047u);
      __builtin_amdgcn_raw_buffer_store_b128((v4u){tag, e.x, tag, e.y}, rws, trow * 4096 + gg * 512 + r32 * 16, WS_HROWS, AUX_SC1);
    }
    SB();
#pragma unroll
    for (int m = 0; m < 4; ++m) {
      out0[m] = pk2(hv[2 * m] + bflo(hn[m]), hv[2 * m + 1] + bfhi(hn[m]));
      out1[m] = pk2(hv[8 + 2 * m] + bflo(hn[4 + m]), hv[9 + 2 * m] + bfhi(hn[4 + m]));
    }
    PIN(out0); PIN(out1);
  }
  STAMP(14);
  unsigned K1, K2;
  {
    v4u a, c;
    for (unsigned s = 0;; ++s) {
      asm volatile("" ::: "memory");
      a = __builtin_amdgcn_raw_buffer_load_b128(rws, lane * 16, so_keyr, AUX_SC1);
      c = __builtin_amdgcn_raw_buffer_load_b128(rws, lane * 16 + 1024, so_keyr, AUX_SC1);
      if (__all(a.x == tag && a.z == tag && c.x == tag && c.z == tag) || s >= SPIN_CAP) break;
      __builtin_amdgcn_s_sleep(2);
    }
    const unsigned hi1 = max(a.y, c.y);
    const unsigned lo1 = max(min(a.y, c.y), max(a.w, c.w));
    K1 = __builtin_amdgcn_readfirstlane(umax_wave(hi1));
    K2 = __builtin_amdgcn_readfirstlane(umax_wave(hi1 == K1 ? lo1 : hi1));
  }
  STAMP(15);
  const unsigned t1 = 2047u - (K1 & 2047u), t2 = 2047u - (K2 & 2047u);
  const bool lost = key != K1 && key != K2;
  if (f1 < 172) {
    v4u g0, g1;
    asm volatile("s_barrier" ::: "memory");
    for (unsigned s = 0;; ++s) {
      asm volatile("" ::: "memory");
      g0 = __builtin_amdgcn_raw_buffer_load_b128(rws, w * 1024 + lane * 16, WS_HROWS + t1 * 4096, AUX_SC1);
      g1 = __builtin_amdgcn_raw_buffer_load_b128(rws, w * 1024 + lane * 16, WS_HROWS + t2 * 4096, AUX_SC1);
      if (__all(g0.x == tag && g0.z == tag && g1.x == tag && g1.z == tag) || s >= SPIN_CAP) break;
    }
    LDS_U1(wpriv + lane * 4) = __builtin_amdgcn_perm(g0.w, g0.y, 0x05040100u);
    LDS_U1(wpriv + 256 + lane * 4) = __builtin_amdgcn_perm(g1.w, g1.y, 0x05040100u);
    const unsigned wa = sb + L_FFW + (p4 >> 1) * 8192 + (128 * w + 8 * G + q4) * 16 + (p4 & 1) * 8;
    v4u a[4];
#pragma unroll
    for (int i = 0; i < 4; ++i) a[i] = tr16x2(wa + i * 512, 64);
    WAIT_LGKM();
    f32x4 d = {0.f, 0.f, 0.f, 0.f};
#pragma unroll
    for (int i = 0; i < 4; ++i) d = mfma16(a[i], LDS_U4(wpriv + (lane & 1) * 256 + i * 64 + G * 16), d);
    if (r16 < 2) LDS_F4(sb + L_RED + (w * 2 + r16) * 64 + G * 16) = d;
    BAR();
    STAMP(16);
    if (tid < 16) {
      const int sel = tid >> 3, i = tid & 7;
      float gs = 0.f, us = 0.f;
#pragma unroll
      for (int j = 0; j < 4; ++j) {
        gs += LDS_F1(sb + L_RED + (j * 2 + sel) * 64 + i * 4);
        us += LDS_F1(sb + L_RED + (j * 2 + sel) * 64 + 32 + i * 4);
      }
      gs = rbf(gs); us = rbf(us);
      const float si = rbf(gs * __builtin_amdgcn_rcpf(1.f + __expf(-gs)));
      const unsigned av = pk2(si * us, 0.f) & 0xffffu;
      const unsigned nb = dppu<0xB1>(av);
      if ((tid & 1) == 0)
        __hip_atomic_store((GAS u64*)(p.ws + WS_AGRAN + f1 * 64 + (tid >> 1) * 8), (u64)tag | ((u64)(av | (nb << 16)) << 32), RLX_AGENT);
    }
    STAMP(17);
  }
  if (lost) {
    __builtin_amdgcn_raw_buffer_store_b128(out0, rout, (unsigned)(tok * 1024 + col * 2), 0, AUX_SC1);
    __builtin_amdgcn_raw_buffer_store_b128(out1, rout, (unsigned)(tok * 1024 + col * 2 + 64), 0, AUX_SC1);
  }
  if (b == 255 && tid == 0) __hip_atomic_store((GAS unsigned*)(p.ws + WS_EPOCH), tag, RLX_AGENT);
  if (f2 >= 0) {
    const int ob = f2;
    asm volatile("s_barrier" ::: "memory");
    v2u hg = {0u, 0u};
    if (tid < 32) {
      const GAS u64* hp = (const GAS u64*)(p.ws + WS_HROWS + ((tid >> 4) ? t2 : t1) * 4096 + (16 * ob + (tid & 15)) * 8);
      for (unsigned s = 0; s < SPIN_CAP; ++s) {
        const u64 gq = __hip_atomic_load(hp, RLX_AGENT);
        hg = (v2u){(unsigned)gq, (unsigned)(gq >> 32)};
        if (__all(hg.x == tag)) break;
      }
    }
    v4u a[11];
    const unsigned wa = sb + L_FFW + (32 * w + 8 * G + q4) * 32 + p4 * 8;
#pragma unroll
    for (int i = 0; i < 11; ++i) a[i] = tr16x2(wa + ((i == 10 && w == 3) ? 0 : i * 4096), 128);
    if (w == 3) a[10] = (v4u){0u, 0u, 0u, 0u};
#pragma unroll
    for (int i = 0; i < 11; ++i) PIN(a[i]);
    const int ksC = w + 4 * (G + 8);
    const unsigned oc = (ksC < 43 ? ksC : w) * 256 + r16 * 16;
    v4u ga, gb, gc;
    for (unsigned s = 0;; ++s) {
      asm volatile("" ::: "memory");
      ga = __builtin_amdgcn_raw_buffer_load_b128(rws, (w + 4 * G) * 256 + r16 * 16, WS_AGRAN, AUX_SC1);
      gb = __builtin_amdgcn_raw_buffer_load_b128(rws, (w + 4 * G) * 256 + r16 * 16, WS_AGRAN + 4096, AUX_SC1);
      gc = __builtin_amdgcn_raw_buffer_load_b128(rws, oc, WS_AGRAN, AUX_SC1);
      if (__all(ga.x == tag && ga.z == tag && gb.x == tag && gb.z == tag && gc.x == tag && gc.z == tag) || s >= SPIN_CAP) break;
    }
    STAMP(16);
    LDS_U2(wpriv + lane * 8) = (v2u){ga.y, ga.w};
    LDS_U2(wpriv + 512 + lane * 8) = (v2u){gb.y, gb.w};
    LDS_U2(wpriv + 1024 + lane * 8) = (v2u){gc.y, gc.w};
    WAIT_LGKM();
    f32x4 d0 = {0.f, 0.f, 0.f, 0.f}, d1 = d0;
#pragma unroll
    for (int i = 0; i < 11; ++i) {
      const v4u bb = LDS_U4(wpriv + i * 128 + G * 32 + (lane & 1) * 16);
      if (i & 1) d1 = mfma16(a[i], bb, d1); else d0 = mfma16(a[i], bb, d0);
    }
    d0 += d1;
    if (r16 < 2) LDS_F4(sb + L_RED + (w * 2 + r16) * 64 + G * 16) = d0;
    BAR();
    if (tid < 32) {
      const int sel = tid >> 4, i = tid & 15;
      float y = 0.f;
#pragma unroll
      for (int j = 0; j < 4; ++j) y += LDS_F1(sb + L_RED + (j * 2 + sel) * 64 + i * 4);
      const unsigned ordk = (sel ? K2 : K1) >> 11;
      const unsigned rb = (ordk & 0x8000u) ? (ordk & 0x7fffu) : (~ordk & 0xffffu);
      const float sg = rbf(__builtin_amdgcn_rcpf(1.f + __expf(-bflo(rb))));
      y = rbf(rbf(y) * sg);
      const float t = rbf(bflo(hg.y) + y);
      const unsigned ov = pk2(bfhi(hg.y) + t, 0.f) & 0xffffu;
      const unsigned nb = dppu<0xB1>(ov);
      if ((tid & 1) == 0)
        __builtin_amdgcn_raw_buffer_store_b32(ov | (nb << 16), rout, (sel ? t2 : t1) * 1024 + (16 * ob + i) * 2, 0, AUX_SC1);
    }
    STAMP(17);
  }
  STAMP(18);
  STAMP_OUT();
}
}

extern "C" void kernel_launch(void* const* d_in, const int* in_sizes, int n_in, void* d_out, int out_size, void* d_ws,
                              size_t ws_size, hipStream_t stream) {
  Params p;
  p.x = (const char*)d_in[0]; p.wqkv = (const char*)d_in[1]; p.wo = (const char*)d_in[2];
  p.wr = (const char*)d_in[4]; p.wg = (const char*)d_in[5]; p.wu = (const char*)d_in[6]; p.wd = (const char*)d_in[7];
  p.out = (char*)d_out; p.ws = (char*)d_ws;
  (void)hipFuncSetAttribute((const void*)fused_block_kernel, hipFuncAttributeMaxDynamicSharedMemorySize, L_TOTAL);
  hipLaunchKernelGGL(fused_block_kernel, dim3(256), dim3(512), L_TOTAL, stream, p);
}
```

```cpp
#include <hip/hip_runtime.h>
#include <hip/hip_bf16.h>

typedef unsigned long long u64;
typedef unsigned v4u __attribute__((ext_vector_type(4)));
typedef unsigned v2u __attribute__((ext_vector_type(2)));
typedef float f32x2 __attribute__((ext_vector_type(2)));
typedef float f32x4 __attribute__((ext_vector_type(4)));
typedef float f32x16 __attribute__((ext_vector_type(16)));
typedef __bf16 bf16x2 __attribute__((ext_vector_type(2)));
typedef __bf16 bf16x4 __attribute__((ext_vector_type(4)));
typedef __bf16 bf16x8 __attribute__((ext_vector_type(8)));
#define GAS __attribute__((address_space(1)))
#define LAS __attribute__((address_space(3)))
#define DI __device__ __forceinline__
#define RLX_AGENT __ATOMIC_RELAXED, __HIP_MEMORY_SCOPE_AGENT

#define STAMPS 0

namespace {
constexpr int FF = 1376;
constexpr int AUX_SC1 = 16;
constexpr unsigned SPIN_CAP = 1u << 16;
constexpr unsigned WS_EPOCH = 0;
constexpr unsigned WS_KVFLAG = 4096;
constexpr unsigned WS_OFLAG = WS_KVFLAG + 256 * 128;
constexpr unsigned WS_KEYS = WS_OFLAG + 512 * 128;
constexpr unsigned WS_AGRAN = WS_KEYS + 256 * 128;
constexpr unsigned WS_STATS = 1u << 18;
constexpr unsigned WS_STAMP = 1u << 19;
constexpr unsigned WS_HROWS = 1u << 20;
constexpr unsigned WS_O = 2u << 20;
constexpr unsigned WS_KV = 4u << 20;
constexpr unsigned WS_BYTES = 8u << 20;
static_assert(WS_AGRAN + 192 * 128 <= WS_STATS && WS_STATS + 128 * 2048 <= WS_STAMP, "ws map");
constexpr int L_X = 0;
constexpr int L_RING = 65536;
constexpr int L_MISC = L_RING + 5 * 16384;
constexpr int L_TAB = L_MISC;
constexpr int L_XS = L_TAB + 1024;
constexpr int L_CS = L_XS + 1024;
constexpr int L_RED = L_CS + 1536;
constexpr int L_QX = L_MISC + 4096;
constexpr int L_WR = L_QX + 8192;
constexpr int L_JUNK = L_WR + 1024;
constexpr int L_SEL = L_RED + 512 - 16;
constexpr int L_FLG = L_RED;
constexpr int L_KEY = L_RED + 64;
constexpr int L_KVX = L_RING + 2 * 16384;
constexpr int L_FFW = 0;
constexpr int L_PRIV = 49152;
constexpr int L_TOTAL = 163840;
static_assert(L_JUNK + 1024 <= L_TOTAL, "lds map");

#define THR1 12
#define THR2 12
#define OSLEEP 2
#define ASLEEP 24
#define GSLEEP 8
#define FB_LIMIT 1048576.f
struct Params {
  const char* x; const char* wqkv; const char* wo; const char* wr; const char* wg; const char* wu; const char* wd;
  char* out; char* ws;
};

DI float asf(unsigned u) { return __builtin_bit_cast(float, u); }
DI unsigned asu(float f) { return __builtin_bit_cast(unsigned, f); }
DI float bflo(unsigned u) { return asf(u << 16); }
DI float bfhi(unsigned u) { return asf(u & 0xffff0000u); }
DI unsigned pk2(float a, float b) { f32x2 f = {a, b}; return __builtin_bit_cast(unsigned, __builtin_convertvector(f, bf16x2)); }
DI float rbf(float a) { return bflo(pk2(a, a)); }
DI float dot2(unsigned a, unsigned b, float c) {
  return __builtin_amdgcn_fdot2_f32_bf16(__builtin_bit_cast(bf16x2, a), __builtin_bit_cast(bf16x2, b), c, false);
}
DI f32x16 mfma32(v4u a, v4u b, f32x16 c) {
  return __builtin_amdgcn_mfma_f32_32x32x16_bf16(__builtin_bit_cast(bf16x8, a), __builtin_bit_cast(bf16x8, b), c, 0, 0, 0);
}
DI f32x4 mfma16(v4u a, v4u b, f32x4 c) {
  return __builtin_amdgcn_mfma_f32_16x16x32_bf16(__builtin_bit_cast(bf16x8, a), __builtin_bit_cast(bf16x8, b), c, 0, 0, 0);
}
DI float sum32(float x) {
  auto r = __builtin_amdgcn_permlane32_swap(asu(x), asu(x), false, false);
  const unsigned r0 = r[0], r1 = r[1];
  return asf(r0) + asf(r1);
}
DI float sum16(float x) {
  auto r = __builtin_amdgcn_permlane16_swap(asu(x), asu(x), false, false);
  const unsigned r0 = r[0], r1 = r[1];
  return asf(r0) + asf(r1);
}
DI float fmax32(float x) {
  auto r = __builtin_amdgcn_permlane32_swap(asu(x), asu(x), false, false);
  const unsigned r0 = r[0], r1 = r[1];
  return fmaxf(asf(r0), asf(r1));
}
template <int C> DI unsigned dppu(unsigned k) { return (unsigned)__builtin_amdgcn_update_dpp(0, (int)k, C, 0xF, 0xF, true); }
template <int C> DI float dppf(float k) { return asf(dppu<C>(asu(k))); }
DI unsigned umax_row16(unsigned k) {
  k = max(k, dppu<0xB1>(k)); k = max(k, dppu<0x4E>(k)); k = max(k, dppu<0x141>(k)); k = max(k, dppu<0x140>(k));
  return k;
}
DI unsigned umax_wave(unsigned k) {
  k = umax_row16(k);
  { auto r = __builtin_amdgcn_permlane16_swap(k, k, false, false); const unsigned r0 = r[0], r1 = r[1]; k = max(r0, r1); }
  { auto r = __builtin_amdgcn_permlane32_swap(k, k, false, false); const unsigned r0 = r[0], r1 = r[1]; k = max(r0, r1); }
  return k;
}
DI float fsum8(float v) { v += dppf<0xB1>(v); v += dppf<0x4E>(v); v += dppf<0x141>(v); return v; }
DI float fsum_row16(float v) { v = fsum8(v); v += dppf<0x140>(v); return v; }
DI void ln_scalars(float s1, float s2, float& mb, float& rs) {
  const float m = s1 * (1.f / 512.f);
  const float var = fmaxf(fmaf(-m, m, s2 * (1.f / 512.f)), 0.f);
  mb = rbf(m);
  rs = __builtin_amdgcn_rcpf(rbf(__builtin_amdgcn_sqrtf(rbf(rbf(var) + 1e-5f))));
}
DI void glds_chunk(unsigned v0, unsigned v1, const char* ba, const char* bb, unsigned m0v) {
  asm volatile("s_mov_b32 m0, %4\n\ts_nop 0\n\tglobal_load_lds_dwordx4 %0, %2\n\t"
               "s_add_u32 m0, m0, 0x1000\n\ts_nop 0\n\tglobal_load_lds_dwordx4 %1, %2\n\t"
               "s_add_u32 m0, m0, 0x1000\n\ts_nop 0\n\tglobal_load_lds_dwordx4 %0, %3\n\t"
               "s_add_u32 m0, m0, 0x1000\n\ts_nop 0\n\tglobal_load_lds_dwordx4 %1, %3"
               :: "v"(v0), "v"(v1), "s"(ba), "s"(bb), "s"(m0v) : "memory", "scc");
}
DI void glds2(unsigned v, const char* ba, const char* bb, unsigned m0v) {
  asm volatile("s_mov_b32 m0, %3\n\ts_nop 0\n\tglobal_load_lds_dwordx4 %0, %1\n\t"
               "s_add_u32 m0, m0, 0x1000\n\ts_nop 0\n\tglobal_load_lds_dwordx4 %0, %2"
               :: "v"(v), "s"(ba), "s"(bb), "s"(m0v) : "memory", "scc");
}
template <int N> DI void wait_vm() { asm volatile("s_waitcnt vmcnt(%0)" :: "n"(N) : "memory"); }
DI void glds1(unsigned v, const char* ba, unsigned m0v) {
  asm volatile("s_mov_b32 m0, %2\n\ts_nop 0\n\tglobal_load_lds_dwordx4 %0, %1" :: "v"(v), "s"(ba), "s"(m0v) : "memory");
}
#define WAIT_VM(n) asm volatile("s_waitcnt vmcnt(" #n ")" ::: "memory")
#define WAIT_LGKM() asm volatile("s_waitcnt lgkmcnt(0)" ::: "memory")
#define BAR() do { __builtin_amdgcn_sched_barrier(0); asm volatile("s_waitcnt lgkmcnt(0)\n\ts_barrier" ::: "memory"); __builtin_amdgcn_sched_barrier(0); } while (0)
#define PIN(x) asm volatile("" : "+v"(x))
#define SPIN_(x) asm volatile("" : "+s"(x))
#define SB() __builtin_amdgcn_sched_barrier(0)
#define LDS_U4(off) (*(LAS v4u*)(size_t)(off))
#define LDS_F4(off) (*(LAS f32x4*)(size_t)(off))
#define LDS_U2(off) (*(LAS v2u*)(size_t)(off))
#define LDS_F1(off) (*(LAS float*)(size_t)(off))
#define LDS_U1(off) (*(LAS unsigned*)(size_t)(off))
DI v2u tr16(unsigned off) { return __builtin_bit_cast(v2u, __builtin_amdgcn_ds_read_tr16_b64_v4bf16((LAS bf16x4*)(size_t)off)); }
DI v4u tr16x2(unsigned off, unsigned d) { const v2u a = tr16(off), b = tr16(off + d); return (v4u){a.x, a.y, b.x, b.y}; }
DI unsigned read_tag(const char* ws) {
  unsigned e;
  asm volatile("s_load_dword %0, %1, 0x0\n\ts_waitcnt lgkmcnt(0)" : "=s"(e) : "s"(ws) : "memory");
  return e + 1u;
}
DI v4u pack8(const float* o) { return (v4u){pk2(o[0], o[1]), pk2(o[2], o[3]), pk2(o[4], o[5]), pk2(o[6], o[7])}; }

#define STAMP(i) do {} while (0)
#define STAMP_OUT() do {} while (0)

#define STAMP2(i) do {} while (0)
constexpr unsigned ONE2 = 0x3F803F80u;
struct Frags { v4u x[4], a[4], b[4]; };
#define JUNKAT 6
constexpr int npieces(int n) { return (n < 8 ? 6 : (n < 12 ? 4 : 0)) + (n == JUNKAT ? 7 : 0); }
#define PRO 2
constexpr int last_before(int j) { const int r = PRO - 1 + 2 * j; return r < j + 3 ? r : j + 3; }
constexpr int younger(int j) { int s = 0; for (int n = j + 1; n <= last_before(j); ++n) s += npieces(n); return s; }

DI void lds_arrive(unsigned addr, int lane) {
  if (lane == 0) __hip_atomic_fetch_add((LAS unsigned*)(size_t)addr, 1u, __ATOMIC_RELAXED, __HIP_MEMORY_SCOPE_WORKGROUP);
}
DI void lds_wait4(unsigned addr) {
  for (unsigned s = 0; s < (SPIN_CAP << 4); ++s) {
    const unsigned v = __hip_atomic_load((LAS unsigned*)(size_t)addr, __ATOMIC_RELAXED, __HIP_MEMORY_SCOPE_WORKGROUP);
    if (__builtin_amdgcn_readfirstlane(v) >= 4u) break;
    __builtin_amdgcn_s_sleep(3);
  }
  asm volatile("" ::: "memory");
}
#define RBAR() asm volatile("s_barrier" ::: "memory")

__global__ __launch_bounds__(512, 2) void fused_block_kernel(Params p) {
  extern __shared__ __attribute__((aligned(16))) char smem[];
  const int lane = threadIdx.x & 63;
  const int wv = __builtin_amdgcn_readfirstlane(threadIdx.x >> 6);
  const int w = wv & 3, tid = threadIdx.x & 255;
  const int b = blockIdx.x, xcd = b & 7, slot = b >> 3;
  const int tt = (xcd & 3) * 8 + (slot & 7), gg = (xcd >> 2) * 4 + (slot >> 3);
  const unsigned sb = (unsigned)(size_t)(LAS char*)smem;
  const int rot = (tt + 2 * gg) & 7;
  const int f1 = slot < 22 ? xcd * 22 + slot : 999;
  const int f2 = (slot >= 22 && slot < 26) ? xcd * 4 + slot - 22 : -1;
  const int r32 = lane & 31, h = lane >> 5, r16 = lane & 15, G = lane >> 4, q4 = r16 >> 2, p4 = lane & 3;
  const int wrow = tid >> 3;
  const unsigned myblk = (unsigned)(tt * 8 + gg);
  const unsigned ov0 = (unsigned)(wrow * 1024 + gg * 128 + (((tid & 7) ^ (((wrow >> 1) & 3) << 1)) << 4)), ov1 = ov0 + 32 * 1024;
  auto issue_wo = [&](int qn) {
    const char* s0 = p.wo + (size_t)(qn * 128) * 1024;
    glds_chunk(ov0, ov1, s0, s0 + 64 * 1024, sb + L_RING + w * 1024 + ((qn + 3) % 5) * 16384);
  };

  if (__builtin_expect(wv < 4, 1)) {
    {
      const unsigned xv = (unsigned)(wrow * 1024 + (((tid & 7) ^ ((wrow >> 1) & 7)) << 4));
      const unsigned wv0 = (unsigned)(wrow * 3072 + gg * 128 + (((tid & 7) ^ (((wrow >> 1) & 1) << 2)) << 4)), wv1 = wv0 + 32 * 3072;
      const unsigned ringw = sb + L_RING + w * 1024;
      const char* xb = p.x + (size_t)tt * 65536;
      auto issue = [&](int n) {
        if (n == JUNKAT) {
          const int u = b * 4 + w, u2 = 1024 + (u % 352);
          glds1(ov0, p.wo + (size_t)(tt & 7) * 65536, sb + L_JUNK);
          glds1(ov1, p.wo + (size_t)(tt & 7) * 65536, sb + L_JUNK);
          glds1((unsigned)lane * 16, p.wg + (size_t)u * 1024, sb + L_JUNK);
          glds1((unsigned)lane * 16, p.wu + (size_t)u * 1024, sb + L_JUNK);
          glds1((unsigned)lane * 16, p.wd + (size_t)u * 1024, sb + L_JUNK);
          glds1((unsigned)lane * 16, (u < 352 ? p.wg : u < 704 ? p.wu : p.wd) + (size_t)u2 * 1024, sb + L_JUNK);
          glds1((unsigned)lane * 16, p.wd + (size_t)(1024 + 320 + (u & 31)) * 1024, sb + L_JUNK);
        }
        if (n < 8) {
          const int c = (n + rot) & 7;
          glds2(xv, xb + c * 128, xb + c * 128 + 32768, sb + L_X + n * 8192 + w * 1024);
          const char* s0 = p.wqkv + (size_t)(c * 64) * 3072 + 1024;
          glds_chunk(wv0, wv1, s0, s0 + 1024, ringw + (n % 5) * 16384);
        } else if (n < 12) {
          glds_chunk(wv0, wv1, p.wqkv + (size_t)(((2 * n + rot) & 7) * 64) * 3072, p.wqkv + (size_t)(((2 * n + 1 + rot) & 7) * 64) * 3072,
                     ringw + (n % 5) * 16384);
        }
      };
      asm volatile(".Lfb_codeA:");
      STAMP(0);
#pragma unroll
      for (int n = 0; n < PRO; ++n) issue(n);
      STAMP(19);
#define LSTEP(j) do { STAMP2(48 + (j)); wait_vm<younger(j)>(); STAMP2(32 + (j)); RBAR(); \
        _Pragma("unroll") for (int n = last_before(j) + 1; n <= last_before((j) + 1); ++n) issue(n); } while (0)
      LSTEP(0); LSTEP(1); LSTEP(2); LSTEP(3); LSTEP(4); LSTEP(5); LSTEP(6); LSTEP(7);
    }
    const unsigned tag = read_tag(p.ws);
    const __amdgpu_buffer_rsrc_t rws = __builtin_amdgcn_make_buffer_rsrc(p.ws, 0, WS_BYTES, 0x00020000);
    RBAR();
    unsigned so_ow = WS_O + (tt * 64 + gg * 2 + (w >> 1)) * 1024, so_ofw = WS_OFLAG + (tt * 16 + gg) * 128, so_kvf = WS_KVFLAG + myblk * 128;
    unsigned sot[4];
#pragma unroll
    for (int t = 0; t < 4; ++t) { sot[t] = WS_KV + (tt - 4 + t >= 0 ? myblk - (4 - t) * 8 : myblk) * 16384; SPIN_(sot[t]); }
    SPIN_(so_ow); SPIN_(so_ofw); SPIN_(so_kvf);
    unsigned kvfo;
    {
      const int st = tt - 4 + (lane & 3);
      kvfo = WS_KVFLAG + ((st < 0 ? tt : st) * 8 + gg) * 128;
    }
    unsigned lo = w * 1024 + lane * 16, lo1 = lo + 4096, lo2 = lo + 8192, lo3 = lo + 12288;
    unsigned a_q = sb + L_QX + lo, a_kv = sb + L_KVX + lo;
    unsigned ooff0 = ((unsigned)(r32 >> 4) * 16) * 1024 + (r16 + 16 * ((w & 1) * 2 + h)) * 16, ooff1 = ooff0 + 32768;
    PIN(kvfo); PIN(lo); PIN(lo1); PIN(lo2); PIN(lo3); PIN(a_q); PIN(a_kv); PIN(ooff0); PIN(ooff1);
    wait_vm<8>(); RBAR();
    unsigned pm[8];
#pragma unroll
    for (int i = 0; i < 8; ++i) {
      const int k0 = ((2 * i) & 3) + 8 * (i >> 1) + 4 * h;
      pm[i] = (k0 <= r32 ? 0xffffu : 0u) | (k0 + 1 <= r32 ? 0xffff0000u : 0u);
      PIN(pm[i]);
    }
    u64 vis[16];
#pragma unroll
    for (int i = 0; i < 16; ++i) {
      const int c0 = (i & 3) + 8 * (i >> 2);
      vis[i] = (u64)(0xffffffffu << c0) | ((u64)(0xffffffffu << (c0 + 4)) << 32);
      SPIN_(vis[i]);
    }
    auto sel = [&](int i, float yes, float no) {
      float r;
      asm("v_cndmask_b32_e64 %0, %1, %2, %3" : "=v"(r) : "v"(no), "v"(yes), "s"(vis[i]));
      return r;
    };
    const int first = 8 - 2 * tt;
    f32x16 cz;
    auto fill = [&](f32x16& blk, float v) {
      f32x2 v2 = {v, v};
      PIN(v2);
#pragma unroll
      for (int i = 0; i < 16; i += 2) { blk[i] = v2[0]; blk[i + 1] = v2[1]; }
      PIN(blk);
    };
    fill(cz, first > 0 ? -1e30f : 0.f);
    v4u kf[10], va[10], vb[10], qf[2];
#pragma unroll
    for (int k = 0; k < 10; ++k) { vb[k] = (v4u){ONE2, ONE2, ONE2, ONE2}; PIN(vb[k]); }
    WAIT_VM(0); RBAR();
    if (threadIdx.x == 0) __builtin_amdgcn_raw_buffer_store_b32(tag, rws, 0, so_kvf, AUX_SC1);
    issue_wo(0); issue_wo(1);
    STAMP(3);
    RBAR();
    unsigned fl = __builtin_amdgcn_raw_buffer_load_b32(rws, kvfo, 0, AUX_SC1);
    kf[8] = LDS_U4(a_kv); kf[9] = LDS_U4(a_kv + 8192);
    va[8] = LDS_U4(a_kv + 4096); va[9] = LDS_U4(a_kv + 12288);
    auto vfix = [&](int k) {
#pragma unroll
      for (int j = 0; j < 4; ++j) {
        auto r = __builtin_amdgcn_permlane16_swap(va[k][j], vb[k][j], false, false);
        const unsigned r0 = r[0], r1 = r[1];
        va[k][j] = r0; vb[k][j] = r1;
      }
      PIN(va[k]); PIN(vb[k]);
    };
    vfix(8); vfix(9);
    SB();
    for (unsigned s = 0; s < SPIN_CAP; ++s) {
      if (__all(fl == tag)) break;
      __builtin_amdgcn_s_sleep(1);
      asm volatile("" ::: "memory");
      fl = __builtin_amdgcn_raw_buffer_load_b32(rws, kvfo, 0, AUX_SC1);
    }
    asm volatile("" ::: "memory");
    STAMP(4);
#pragma unroll
    for (int k = 0; k < 8; ++k) kf[k] = __builtin_amdgcn_raw_buffer_load_b128(rws, (k & 1) ? lo2 : lo, sot[k >> 1], AUX_SC1);
    SB();
    RBAR();
    qf[0] = LDS_U4(a_q); qf[1] = LDS_U4(a_q + 4096);
#pragma unroll
    for (int k = 0; k < 8; ++k) va[k] = __builtin_amdgcn_raw_buffer_load_b128(rws, (k & 1) ? lo3 : lo1, sot[k >> 1], AUX_SC1);
    SB();
    RBAR();
    auto oflag = [&](int qb) {
      WAIT_VM(0);
      if (lane == 0) {
        const unsigned n = __hip_atomic_fetch_add((LAS unsigned*)(size_t)(sb + L_FLG + 8 + qb * 4), 1u, __ATOMIC_RELAXED, __HIP_MEMORY_SCOPE_WORKGROUP);
        if (n == 3u) __builtin_amdgcn_raw_buffer_store_b32(tag, rws, qb * 1024, so_ofw, AUX_SC1);
      }
    };
    STAMP(5);
    f32x16 zero;
#pragma unroll
    for (int i = 0; i < 16; ++i) zero[i] = 0.f;
    auto mx16 = [&](float& ma, float& mb, const f32x16& s) {
#pragma unroll
      for (int i = 0; i < 16; i += 4) { ma = fmaxf(fmaxf(ma, s[i]), s[i + 1]); mb = fmaxf(fmaxf(mb, s[i + 2]), s[i + 3]); }
      PIN(ma); PIN(mb);
    };
    {
      f32x16 sm[2];
      sm[0] = mfma32(kf[8], qf[0], zero); PIN(sm[0]);
      f32x16 s0 = mfma32(kf[0], qf[0], cz); PIN(s0);
      sm[1] = mfma32(kf[9], qf[1], zero); PIN(sm[1]);
      f32x16 s1 = mfma32(kf[1], qf[1], cz); PIN(s1);
      asm volatile("s_nop 7\n\ts_nop 1");
      SB();
#pragma unroll
      for (int i = 0; i < 16; ++i) sm[0][i] = sel(i, sm[0][i], s0[i]);
#pragma unroll
      for (int i = 0; i < 16; ++i) sm[1][i] = sel(i, sm[1][i], s1[i]);
      SB();
      float m0, m1;
      {
        float a0 = -3e38f, b0 = -3e38f, a1 = -3e38f, b1 = -3e38f;
        mx16(a0, b0, sm[0]); mx16(a1, b1, sm[1]);
        auto r = __builtin_amdgcn_permlane32_swap(asu(fmaxf(a0, b0)), asu(fmaxf(a1, b1)), false, false);
        const unsigned r0 = r[0], r1 = r[1];
        const float mm = fmaxf(asf(r0), asf(r1));
        auto q = __builtin_amdgcn_permlane32_swap(asu(mm), asu(mm), false, false);
        const unsigned q0 = q[0], q1 = q[1];
        m0 = asf(q0); m1 = asf(q1);
      }
#pragma unroll
      for (int k = 0; k < 8; ++k) vfix(k);
      STAMP(6);
#pragma unroll
      for (int qb = 0; qb < 2; ++qb) {
        float mx = qb ? m1 : m0;
        f32x16 o;
#pragma unroll 1
        for (int att = 0;; ++att) {
          v4u pp;
          fill(cz, first > 1 + qb ? -1e30f : -mx);
          SB();
          s0 = mfma32(kf[1 + qb], qf[qb], cz);
          PIN(s0);
          SB();
          {
            float e[16];
#pragma unroll
            for (int i = 0; i < 16; ++i) e[i] = __builtin_amdgcn_exp2f(sm[qb][i] - mx);
            const v4u p0 = pack8(e), p1 = pack8(e + 8);
            v4u d0, d1, f0, f1_;
#pragma unroll
            for (int j = 0; j < 4; ++j) { d0[j] = p0[j] & pm[j]; f0[j] = p0[j] ^ d0[j]; d1[j] = p1[j] & pm[4 + j]; f1_[j] = p1[j] ^ d1[j]; }
            o = mfma32(va[8 + qb], d0, zero);
            o = mfma32(va[qb], f0, o);
            o = mfma32(vb[8 + qb], d1, o);
            pp = f1_;
            PIN(pp); PIN(o);
          }
          SB();
#pragma unroll
          for (int k = 1 + qb; k < 8 + qb; ++k) {
            float e[16];
            if (k + 1 < 8 + qb && (k & 1) == 1 && __builtin_expect(first == k + 1, 0)) fill(cz, -mx);
#pragma unroll
            for (int i = 0; i < 4; ++i) e[i] = __builtin_amdgcn_exp2f(s0[i]);
            SB();
            o = mfma32(vb[k - 1], pp, o);
            PIN(o);
            SB();
#pragma unroll
            for (int i = 4; i < 8; ++i) e[i] = __builtin_amdgcn_exp2f(s0[i]);
            SB();
            if (k + 1 < 8 + qb) { s1 = mfma32(kf[k + 1], qf[qb], cz); PIN(s1); }
            SB();
            v4u p0 = pack8(e);
            PIN(p0);
            SB();
            e[8] = __builtin_amdgcn_exp2f(s0[8]); e[9] = __builtin_amdgcn_exp2f(s0[9]);
            SB();
            o = mfma32(va[k], p0, o);
            PIN(o);
            SB();
#pragma unroll
            for (int i = 10; i < 16; ++i) e[i] = __builtin_amdgcn_exp2f(s0[i]);
            pp = pack8(e + 8);
            PIN(pp);
            if (qb == 1 && k == 5 && att == 0) oflag(0);
            SB();
            s0 = s1;
          }
          o = mfma32(vb[7 + qb], pp, o);
          if (__builtin_expect(__all(o[8] < FB_LIMIT) || att == 1, 1)) break;
          float ta = mx, tb_ = mx;
#pragma unroll
          for (int k = 1 + qb; k < 8 + qb; ++k)
            if (k >= first) mx16(ta, tb_, mfma32(kf[k], qf[qb], zero));
          mx = fmax32(fmaxf(ta, tb_));
        }
        const float inv = __builtin_amdgcn_rcpf(o[8]);
        float on[8];
#pragma unroll
        for (int i = 0; i < 8; ++i) on[i] = o[i] * inv;
        __builtin_amdgcn_raw_buffer_store_b128(pack8(on), rws, qb ? ooff1 : ooff0, so_ow, AUX_SC1);
      }
    }
    STAMP(7);
    oflag(1);
    STAMP(8);
    if (w != 0) { STAMP_OUT(); return; }
    v4u a[16];
    if (f1 < 172) {
      lds_wait4(sb + L_FLG);
      const unsigned wa = sb + L_FFW + (p4 >> 1) * 8192 + (8 * G + q4) * 16 + (p4 & 1) * 8;
#pragma unroll
      for (int i = 0; i < 16; ++i) a[i] = tr16x2(wa + i * 512, 64);
#pragma unroll
      for (int i = 0; i < 16; ++i) PIN(a[i]);
    }
    unsigned so_keyr = WS_KEYS + xcd * 4096, so_key = WS_KEYS + tt * 128, kro = r32 * 128, l16 = lane * 16, st_w = sb + L_PRIV + 8192 + lane * 4, st_r = sb + L_PRIV + 8192 + (lane & 1) * 1024 + G * 16;
    SPIN_(so_keyr); SPIN_(so_key); PIN(kro); PIN(l16); PIN(st_w); PIN(st_r);
    RBAR();
    if (gg == 0) {
      const unsigned kq = LDS_U1(sb + L_KEY + (lane & 7) * 4);
      unsigned k1 = max(kq, dppu<0xB1>(kq)); k1 = max(k1, dppu<0x4E>(k1)); k1 = max(k1, dppu<0x141>(k1));
      unsigned k2 = kq == k1 ? 0u : kq; k2 = max(k2, dppu<0xB1>(k2)); k2 = max(k2, dppu<0x4E>(k2)); k2 = max(k2, dppu<0x141>(k2));
      if (lane < 8) __builtin_amdgcn_raw_buffer_store_b128((v4u){tag, k1, tag, k2}, rws, lane * 4096, so_key, AUX_SC1);
    }
    unsigned K1, K2;
    {
      v4u ka;
      for (unsigned s = 0;; ++s) {
        asm volatile("" ::: "memory");
        ka = __builtin_amdgcn_raw_buffer_load_b128(rws, kro, so_keyr, AUX_SC1);
        if (__all(ka.x == tag && ka.z == tag) || s >= SPIN_CAP) break;
        __builtin_amdgcn_s_sleep(2);
      }
      const unsigned kk = h ? ka.w : ka.y;
      K1 = __builtin_amdgcn_readfirstlane(umax_wave(kk));
      K2 = __builtin_amdgcn_readfirstlane(umax_wave(kk == K1 ? 0u : kk));
    }
    STAMP(15);
    const unsigned g1 = (2047u - (K1 & 2047u)) >> 4, g2 = (2047u - (K2 & 2047u)) >> 4;
    const unsigned r1 = WS_HROWS + g1 * 8192, r2 = WS_HROWS + g2 * 8192 + (g1 == g2 ? 4096 : 0);
    v4u g[8];
    if (f1 < 172) {
#pragma unroll
      for (int i = 0; i < 8; ++i) g[i] = __builtin_amdgcn_raw_buffer_load_b128(rws, l16 + (i & 3) * 1024, i < 4 ? r1 : r2, AUX_SC1);
    }
    if (lane == 0) LDS_U2(sb + L_SEL) = (v2u){K1, K2};
    asm volatile("s_waitcnt lgkmcnt(0)\n\ts_barrier" ::: "memory");
    if (b == 255 && lane == 0) __builtin_amdgcn_raw_buffer_store_b32(tag, rws, 0, WS_EPOCH, AUX_SC1);
    if (f1 < 172) {
      for (unsigned s = 0;; ++s) {
        bool ok = true;
#pragma unroll
        for (int i = 0; i < 8; ++i) ok = ok && g[i].x == tag && g[i].z == tag;
        if (__all(ok) || s >= SPIN_CAP) break;
        asm volatile("" ::: "memory");
#pragma unroll
        for (int i = 0; i < 8; ++i) g[i] = __builtin_amdgcn_raw_buffer_load_b128(rws, l16 + (i & 3) * 1024, i < 4 ? r1 : r2, AUX_SC1);
      }
#pragma unroll
      for (int i = 0; i < 8; ++i) LDS_U1(st_w + i * 256) = __builtin_amdgcn_perm(g[i].w, g[i].y, 0x05040100u);
      WAIT_LGKM();
      v4u bb[16];
#pragma unroll
      for (int i = 0; i < 16; ++i) bb[i] = LDS_U4(st_r + i * 64);
      SB();
      f32x4 d0 = {0.f, 0.f, 0.f, 0.f}, d1 = d0;
#pragma unroll
      for (int i = 0; i < 16; i += 2) { d0 = mfma16(a[i], bb[i], d0); d1 = mfma16(a[i + 1], bb[i + 1], d1); }
      d0 += d1;
      float av[4];
#pragma unroll
      for (int j = 0; j < 4; ++j) {
        auto r = __builtin_amdgcn_permlane32_swap(asu(d0[j]), asu(d0[j]), false, false);
        const unsigned r0 = r[0], r1_ = r[1];
        const float gs = rbf(asf(r0)), us = rbf(asf(r1_));
        av[j] = rbf(gs * __builtin_amdgcn_rcpf(1.f + __builtin_amdgcn_exp2f(gs * -1.4426950408889634f))) * us;
      }
      if (lane < 32 && r16 < 2)
        __builtin_amdgcn_raw_buffer_store_b128((v4u){tag, pk2(av[0], av[1]), tag, pk2(av[2], av[3])}, rws, r16 * 32 + G * 16, WS_AGRAN + f1 * 128, AUX_SC1);
      STAMP(16);
    }
    STAMP_OUT();
    asm volatile(".Lfb_code2:");
    return;
  }

  STAMP(0);
  {
    const char* pc;
    int lo_, hi_;
    asm volatile("s_getpc_b64 %0\n.Lfb_code0:\n\ts_mov_b32 %1, .Lfb_codeA-.Lfb_code0\n\ts_mov_b32 %2, .Lfb_code1-.Lfb_code0\n\t"
                 "s_max_i32 %2, %2, .Lfb_code2-.Lfb_code0\n\ts_min_i32 %1, %1, 0" : "=s"(pc), "=s"(lo_), "=s"(hi_) :: "scc");
    const int off = min((slot * 4 + w) * 1024, hi_ - lo_ - 1024);
    if ((slot * 4 + w) * 1024 < hi_ - lo_) glds1((unsigned)lane * 16, pc + lo_ + off, sb + L_JUNK);
  }
  if (w == 3) glds1((unsigned)lane * 16, p.wr, sb + L_WR);
  if (tid < 4) LDS_U1(sb + L_FLG + tid * 4) = 0u;
  const unsigned tag = read_tag(p.ws);
  const __amdgpu_buffer_rsrc_t rws = __builtin_amdgcn_make_buffer_rsrc(p.ws, 0, WS_BYTES, 0x00020000);
  const int tb = w & 1, ct = w >> 1;
  unsigned so_kv = WS_KV + myblk * 16384;
  SPIN_(so_kv);
  unsigned xa[4], wa[4], wb[4];
#pragma unroll
  for (int i = 0; i < 4; ++i) {
    const int ks = i ^ (ct | (tb << 1));
    xa[i] = sb + L_X + (tb * 32 + r32) * 128 + (((2 * ks + h) ^ ((r32 >> 1) & 7)) << 4);
    wa[i] = sb + L_RING + (16 * ks + 8 * h + q4) * 128 + ((ct ^ ((q4 >> 1) & 1)) << 6) + (G & 1) * 32 + p4 * 8;
    wb[i] = wa[i] + 49152;
    PIN(xa[i]); PIN(wa[i]); PIN(wb[i]);
  }
  unsigned a_csw = sb + L_CS + h * 512 + w * 128 + r32 * 4, a_xsw = sb + L_XS + w * 256 + r32 * 8 + h * 4;
  unsigned a_xsr = sb + L_XS + tb * 256 + r32 * 8, a_csr = sb + L_CS + ct * 256 + h * 16, a_cvr = sb + L_CS + 512 + ct * 256 + r32 * 4;
  unsigned a_tabw = sb + L_TAB + w * 256 + lane * 4, a_tabr = sb + L_TAB + w * 256 + h * 16, a_cqw = sb + L_CS + 1024 + w * 128 + r32 * 4;
  unsigned ko = tb * 8192 + 2 * ct * 1024 + lane * 16;
  unsigned vo = tb * 8192 + 4096 + (2 * ct + (r32 >> 4)) * 1024 + (h * 32 + r16) * 16;
  unsigned a_kx = sb + L_KVX + ko, a_vx = sb + L_KVX + vo, a_qw = sb + L_QX + (tb * 4 + 2 * ct) * 1024 + lane * 16;
  unsigned a_xres = sb + L_X + ((gg - rot) & 7) * 8192 + (w * 16 + r16) * 128 + (((G >> 1) ^ ((r16 >> 1) & 7)) << 4) + (G & 1) * 8;
  PIN(a_csw); PIN(a_xsw); PIN(a_xsr); PIN(a_csr); PIN(a_cvr); PIN(a_tabw); PIN(a_tabr); PIN(a_cqw);
  PIN(ko); PIN(vo); PIN(a_kx); PIN(a_vx); PIN(a_qw); PIN(a_xres);
  f32x16 acc0, acc1;
#pragma unroll
  for (int i = 0; i < 16; ++i) { acc0[i] = 0.f; acc1[i] = 0.f; }
  float cs0 = 0.f, cs1 = 0.f, xs = 0.f, xq = 0.f;
  auto rd = [&](Frags& f, int slab, int sl) {
#pragma unroll
    for (int i = 0; i < 4; ++i) {
      const unsigned wo = sl < 3 ? wa[i] + sl * 16384 : wb[i] + (sl - 3) * 16384;
      f.x[i] = LDS_U4(xa[i] + slab * 8192); f.a[i] = tr16x2(wo, 512); f.b[i] = tr16x2(wo + 8192, 512);
    }
  };
  auto sums = [&](const Frags& f, bool withx) {
    PIN(acc0);
    if (withx) PIN(acc1);
#pragma unroll
    for (int i = 0; i < 8; ++i) { __builtin_amdgcn_sched_group_barrier(0x8, 1, 0); __builtin_amdgcn_sched_group_barrier(0x100, 3, 0); }
    SB();
#pragma unroll
    for (int j = 0; j < 4; ++j) {
      cs0 = dot2(f.a[0][j], ONE2, cs0); cs1 = dot2(f.b[0][j], ONE2, cs1);
      if (withx) { xs = dot2(f.x[0][j], ONE2, xs); xq = dot2(f.x[0][j], f.x[0][j], xq); }
    }
#pragma unroll
    for (int j = 0; j < 4; ++j) {
      cs0 = dot2(f.a[1][j], ONE2, cs0); cs1 = dot2(f.b[1][j], ONE2, cs1);
      if (withx) { xs = dot2(f.x[2][j], ONE2, xs); xq = dot2(f.x[2][j], f.x[2][j], xq); }
    }
    PIN(cs0); PIN(cs1);
    if (withx) { PIN(xs); PIN(xq); }
    SB();
  };
  auto mmA = [&](const Frags& f) {
#pragma unroll
    for (int i = 0; i < 4; ++i) { acc0 = mfma32(f.a[i], f.x[i], acc0); acc1 = mfma32(f.x[i], f.b[i], acc1); }
  };
  Frags fa, fb2;
  PIN(acc0); PIN(acc1);
  STAMP2(32); BAR(); rd(fa, 0, 0); sums(fa, true);
  STAMP(20);
  STAMP2(33); BAR(); rd(fb2, 1, 1); mmA(fa); sums(fb2, true);
  STAMP2(34); BAR(); rd(fa, 2, 2); mmA(fb2); sums(fa, true);
  STAMP2(35); BAR(); rd(fb2, 3, 3); mmA(fa); sums(fb2, true);
  STAMP2(36); BAR(); rd(fa, 4, 4); mmA(fb2); sums(fa, true);
  STAMP2(37); BAR(); rd(fb2, 5, 0); mmA(fa); sums(fb2, true);
  STAMP2(38); BAR(); rd(fa, 6, 1); mmA(fb2); sums(fa, true);
  STAMP2(39); BAR(); rd(fb2, 7, 2); mmA(fa); sums(fb2, true);
  {
    auto rc = __builtin_amdgcn_permlane32_swap(asu(cs0), asu(cs1), false, false);
    auto rx = __builtin_amdgcn_permlane32_swap(asu(xs), asu(xq), false, false);
    const unsigned c0 = rc[0], c1 = rc[1], x0 = rx[0], x1 = rx[1];
    LDS_F1(a_csw) = asf(c0) + asf(c1);
    LDS_F1(a_xsw) = asf(x0) + asf(x1);
  }
  cs0 = 0.f; cs1 = 0.f;
  STAMP(27);
  BAR();
  float rs1, mr1;
  v4u xh[4];
  auto rdx = [&](int slab) {
#pragma unroll
    for (int i = 0; i < 4; ++i) xh[i] = LDS_U4(xa[i] + slab * 8192);
  };
  {
    const v2u t0 = LDS_U2(a_xsr), t1 = LDS_U2(a_xsr + 512);
    f32x4 ca[4], cb[4];
#pragma unroll
    for (int g = 0; g < 4; ++g) { ca[g] = LDS_F4(a_csr + g * 32); cb[g] = LDS_F4(a_csr + 128 + g * 32); }
    const float cv0 = LDS_F1(a_cvr), cv1 = LDS_F1(a_cvr + 128);
    SB();
#pragma unroll
    for (int i = 0; i < 4; ++i) acc0 = mfma32(fb2.a[i], fb2.x[i], acc0);
    PIN(acc0);
    float mb;
    ln_scalars(asf(t0.x) + asf(t1.x), asf(t0.y) + asf(t1.y), mb, rs1);
    mr1 = -mb * rs1;
    LDS_F1(a_tabw) = h ? mr1 : rs1;
    f32x4 r4[4], m4[4];
#pragma unroll
    for (int g = 0; g < 4; ++g) { r4[g] = LDS_F4(a_tabr + g * 32); m4[g] = LDS_F4(a_tabr + 128 + g * 32); }
    SB();
#pragma unroll
    for (int i = 0; i < 4; ++i) acc1 = mfma32(fb2.x[i], fb2.b[i], acc1);
    PIN(acc1);
    float o[16];
#pragma unroll
    for (int g = 0; g < 4; ++g)
#pragma unroll
      for (int j = 0; j < 4; ++j) o[4 * g + j] = fmaf(acc0[4 * g + j], rs1, mr1 * (ca[g][j] + cb[g][j]));
    const v4u k0 = pack8(o), k1 = pack8(o + 8);
#pragma unroll
    for (int i = 0; i < 4; ++i) { __builtin_amdgcn_sched_group_barrier(0x8, 1, 0); __builtin_amdgcn_sched_group_barrier(0x2, 6, 0); }
    SB();
    __builtin_amdgcn_raw_buffer_store_b128(k0, rws, ko, so_kv, AUX_SC1);
    __builtin_amdgcn_raw_buffer_store_b128(k1, rws, ko + 1024, so_kv, AUX_SC1);
    SB();
    const float cv = cv0 + cv1;
#pragma unroll
    for (int g = 0; g < 4; ++g)
#pragma unroll
      for (int j = 0; j < 4; ++j) o[4 * g + j] = fmaf(acc1[4 * g + j], r4[g][j], m4[g][j] * cv);
    const v4u v0 = pack8(o), v1 = pack8(o + 8);
    __builtin_amdgcn_raw_buffer_store_b128(v0, rws, vo, so_kv, AUX_SC1);
    __builtin_amdgcn_raw_buffer_store_b128(v1, rws, vo + 256, so_kv, AUX_SC1);
    SB();
    STAMP(1);
    RBAR();
    rd(fa, 0, 3); rdx(1);
    LDS_U4(a_kx) = k0; LDS_U4(a_kx + 1024) = k1;
    LDS_U4(a_vx) = v0; LDS_U4(a_vx + 256) = v1;
  }
#pragma unroll
  for (int i = 0; i < 16; ++i) acc0[i] = 0.f;
  auto mmB = [&](const Frags& f) {
#pragma unroll
    for (int i = 0; i < 4; ++i) acc0 = mfma32(f.b[i], xh[i], acc0);
#pragma unroll
    for (int i = 0; i < 4; ++i) acc0 = mfma32(f.a[i], f.x[i], acc0);
  };
  sums(fa, false);
  rd(fb2, 2, 4); mmB(fa); rdx(3); sums(fb2, false);
  WAIT_VM(0);
  BAR();
  SB();
  STAMP(2);
  rd(fa, 4, 0); mmB(fb2); rdx(5); sums(fa, false);
  rd(fb2, 6, 1); mmB(fa); rdx(7);
  v2u xres[4];
#pragma unroll
  for (int c = 0; c < 4; ++c) xres[c] = LDS_U2(a_xres ^ (c * 32));
  sums(fb2, false);
  cs0 = sum32(cs0 + cs1);
  LDS_F1(a_cqw) = cs0;
  mmB(fb2);
  PIN(acc0);
  BAR();
  {
    f32x4 ca[4], cb[4];
#pragma unroll
    for (int g = 0; g < 4; ++g) { ca[g] = LDS_F4(a_csr + 1024 + g * 32); cb[g] = LDS_F4(a_csr + 1024 + 128 + g * 32); }
    SB();
    float o[16];
    const float sc = 0.25f * 1.4426950408889634f;
#pragma unroll
    for (int g = 0; g < 4; ++g)
#pragma unroll
      for (int j = 0; j < 4; ++j) o[4 * g + j] = fmaf(acc0[4 * g + j], rs1, mr1 * (ca[g][j] + cb[g][j]));
    const v4u a0 = pack8(o), a1 = pack8(o + 8);
#pragma unroll
    for (int j = 0; j < 4; ++j) { o[2 * j] = bflo(a0[j]) * sc; o[2 * j + 1] = bfhi(a0[j]) * sc; o[8 + 2 * j] = bflo(a1[j]) * sc; o[9 + 2 * j] = bfhi(a1[j]) * sc; }
    LDS_U4(a_qw) = pack8(o);
    LDS_U4(a_qw + 1024) = pack8(o + 8);
  }
  BAR();
  STAMP(3);
  const int tok = tt * 64 + w * 16 + r16;
  unsigned so_ofr = WS_OFLAG + (tt * 2 + (w >> 1)) * 1024;
  unsigned so_st = WS_STATS + (tt * 4 + w) * 2048, so_hr = WS_HROWS + (tt * 4 + w) * 8192 + gg * 512;
  unsigned oq[4];
#pragma unroll
  for (int qn = 0; qn < 4; ++qn) { oq[qn] = WS_O + (tt * 4 + w) * 16384 + qn * 4096; SPIN_(oq[qn]); }
  SPIN_(so_ofr); SPIN_(so_st); SPIN_(so_hr);
  unsigned woa[4], wob[4];
#pragma unroll
  for (int c = 0; c < 4; ++c) {
    woa[c] = sb + L_RING + (16 * (G >> 1) + 4 * (G & 1) + q4) * 128 + ((c ^ (2 * (G & 1) + (q4 >> 1))) << 5) + p4 * 8;
    wob[c] = woa[c] + 49152;
    PIN(woa[c]); PIN(wob[c]);
  }
  auto wofrag = [&](int qn, int ks, int c) { return tr16x2((qn < 2 ? wob[c] + qn * 16384 : woa[c] + (qn - 2) * 16384) + ks * 4096, 1024); };
  v2u wr[4];
#pragma unroll
  for (int c = 0; c < 4; ++c) { wr[c] = LDS_U2(sb + L_WR + gg * 128 + G * 8 + c * 32); PIN(wr[c]); }
  const v4u wrall = LDS_U4(sb + L_WR + lane * 16);
  float wrsum;
  {
    float s = 0.f;
#pragma unroll
    for (int j = 0; j < 4; ++j) s = dot2(wrall[j], ONE2, s);
    wrsum = sum32(sum16(fsum_row16(s)));
    PIN(wrsum);
  }
  const __amdgpu_buffer_rsrc_t rout = __builtin_amdgcn_make_buffer_rsrc(p.out, 0, 2048 * 1024, 0x00020000);
  const unsigned wpriv = sb + L_PRIV + w * 2048;
  unsigned l16 = lane * 16, ofo = (lane & 7) * 128, sto = lane * 16 + gg * 256;
  unsigned key, a_cw = wpriv + G * 16, a_cr = wpriv + lane * 8, hro = h * 4096 + r32 * 16, keylo = 2047 - tok;
  unsigned fv = f2 >= 0 ? (unsigned)((lane >> 1) * 1024 + (lane & 1) * 16) : (unsigned)lane * (FF * 2);
  PIN(l16); PIN(ofo); PIN(sto); PIN(a_cw); PIN(a_cr); PIN(hro); PIN(keylo); PIN(fv);
  SB();
  RBAR();
  issue_wo(2); issue_wo(3);
  if (f1 < 172) {
#pragma unroll 1
    for (int i = w * 2; i < w * 2 + 2; ++i) {
      glds1(fv, p.wg + f1 * 16 + (size_t)i * 64 * FF * 2, sb + L_FFW + i * 1024);
      wait_vm<THR1>();
      glds1(fv, p.wu + f1 * 16 + (size_t)i * 64 * FF * 2, sb + L_FFW + 8192 + i * 1024);
      wait_vm<THR1>();
    }
  } else if (f2 >= 0) {
#pragma unroll 1
    for (int i = w; i < 43; i += 4) {
      glds1(fv, p.wd + f2 * 32 + (size_t)i * 32 * 1024, sb + L_FFW + i * 1024);
      wait_vm<THR2>();
    }
  }
  STAMP(21);
  v4u wf[2][4][4];
#pragma unroll
  for (int qn = 0; qn < 2; ++qn)
#pragma unroll
    for (int ks = 0; ks < 4; ++ks)
#pragma unroll
      for (int c = 0; c < 4; ++c) wf[qn][ks][c] = wofrag(qn, ks, c);
#pragma unroll
  for (int qn = 0; qn < 2; ++qn)
#pragma unroll
    for (int ks = 0; ks < 4; ++ks)
#pragma unroll
      for (int c = 0; c < 4; ++c) PIN(wf[qn][ks][c]);
  STAMP(22);
  WAIT_VM(0);
  lds_arrive(sb + L_FLG, lane);
  STAMP(5);
  lds_wait4(sb + L_FLG);
  float hv[16];
  {
    lds_wait4(sb + L_FLG + 8 + (w >> 1) * 4);
    __builtin_amdgcn_s_sleep(OSLEEP);
    for (unsigned s = 0; s < SPIN_CAP; ++s) {
      asm volatile("" ::: "memory");
      const unsigned f = __builtin_amdgcn_raw_buffer_load_b32(rws, ofo, so_ofr, AUX_SC1);
      if (__all(f == tag)) break;
      __builtin_amdgcn_s_sleep(2);
    }
    asm volatile("" ::: "memory");
    STAMP(9);
    v4u of[4][4];
#pragma unroll
    for (int qn = 0; qn < 4; ++qn)
#pragma unroll
      for (int ks = 0; ks < 4; ++ks) of[qn][ks] = __builtin_amdgcn_raw_buffer_load_b128(rws, l16 + ks * 1024, oq[qn], AUX_SC1);
    asm volatile("" ::: "memory");
    SB();
    f32x4 a[4];
#pragma unroll
    for (int c = 0; c < 4; ++c) a[c] = (f32x4){0.f, 0.f, 0.f, 0.f};
#pragma unroll
    for (int qn = 0; qn < 2; ++qn)
#pragma unroll
      for (int ks = 0; ks < 4; ++ks)
#pragma unroll
        for (int c = 0; c < 4; ++c) {
          a[c] = mfma16(wf[qn][ks][c], of[qn][ks], a[c]);
          wf[qn][ks][c] = wofrag(qn + 2, ks, c);
        }
#pragma unroll
    for (int c = 0; c < 4; ++c) PIN(a[c]);
#pragma unroll
    for (int i = 0; i < 32; ++i) { __builtin_amdgcn_sched_group_barrier(0x8, 1, 0); __builtin_amdgcn_sched_group_barrier(0x100, 2, 0); }
    SB();
#pragma unroll
    for (int qn = 0; qn < 2; ++qn)
#pragma unroll
      for (int ks = 0; ks < 4; ++ks)
#pragma unroll
        for (int c = 0; c < 4; ++c) a[c] = mfma16(wf[qn][ks][c], of[qn + 2][ks], a[c]);
    unsigned hb[8];
#pragma unroll
    for (int c = 0; c < 4; ++c)
#pragma unroll
      for (int m = 0; m < 2; ++m) {
        const unsigned xr = xres[c][m];
        const unsigned ab = pk2(a[c][2 * m], a[c][2 * m + 1]);
        hb[2 * c + m] = pk2(bflo(xr) + bflo(ab), bfhi(xr) + bfhi(ab));
      }
    STAMP(11);
    float s1 = 0.f, s2 = 0.f, s3 = 0.f;
#pragma unroll
    for (int m = 0; m < 8; ++m) { s1 = dot2(hb[m], ONE2, s1); s2 = dot2(hb[m], hb[m], s2); s3 = dot2(hb[m], wr[m >> 1][m & 1], s3); }
    s1 = sum32(sum16(s1)); s2 = sum32(sum16(s2)); s3 = sum32(sum16(s3));
    if (lane < 16)
      __builtin_amdgcn_raw_buffer_store_b128((v4u){asu(s1), asu(s2), asu(s3), tag ^ asu(s1) ^ asu(s2) ^ asu(s3)}, rws, sto, so_st, AUX_SC1);
    SB();
#pragma unroll
    for (int m = 0; m < 8; ++m) { hv[2 * m] = bflo(hb[m]); hv[2 * m + 1] = bfhi(hb[m]); PIN(hv[2 * m]); PIN(hv[2 * m + 1]); }
  }
  STAMP(12);
  v4u out0, out1;
  {
    float s1, s2, s3;
    for (unsigned s = 0;; ++s) {
      asm volatile("" ::: "memory");
      const v4u a = __builtin_amdgcn_raw_buffer_load_b128(rws, l16, so_st, AUX_SC1);
      const v4u c = __builtin_amdgcn_raw_buffer_load_b128(rws, l16 + 1024, so_st, AUX_SC1);
      s1 = asf(a.x) + asf(c.x); s2 = asf(a.y) + asf(c.y); s3 = asf(a.z) + asf(c.z);
      if (__all((a.x ^ a.y ^ a.z ^ a.w) == tag && (c.x ^ c.y ^ c.z ^ c.w) == tag) || s >= SPIN_CAP) break;
      __builtin_amdgcn_s_sleep(1);
    }
    STAMP(13);
    s1 = sum32(sum16(s1)); s2 = sum32(sum16(s2)); s3 = sum32(sum16(s3));
    float mb, rs;
    ln_scalars(s1, s2, mb, rs);
    const unsigned rb = pk2((s3 - mb * wrsum) * rs, 0.f) & 0xffffu;
    const unsigned ord = (rb & 0x8000u) ? (~rb & 0xffffu) : (rb | 0x8000u);
    key = (ord << 11) | keylo;
    const unsigned k1 = umax_row16(key);
    const unsigned k2 = umax_row16(key == k1 ? 0u : key);
    if (lane == 0) LDS_U2(sb + L_KEY + w * 8) = (v2u){k1, k2};
    BAR();
    unsigned hn[8];
#pragma unroll
    for (int m = 0; m < 8; ++m) {
      const unsigned d = pk2(hv[2 * m] - mb, hv[2 * m + 1] - mb);
      hn[m] = pk2(bflo(d) * rs, bfhi(d) * rs);
    }
    if (key == k1 || key == k2) {
      const unsigned base = a_cw + (key == k1 ? 0 : 256);
#pragma unroll
      for (int q = 0; q < 4; ++q) {
        const unsigned a0 = hn[2 * q], a1 = hn[2 * q + 1];
        LDS_U4(base + q * 64) = (v4u){(a0 & 0xffffu) | asu(hv[4 * q]), (a0 >> 16) | asu(hv[4 * q + 1]),
                                      (a1 & 0xffffu) | asu(hv[4 * q + 2]), (a1 >> 16) | asu(hv[4 * q + 3])};
      }
    }
    WAIT_LGKM();
    {
      const v2u e = LDS_U2(a_cr);
      __builtin_amdgcn_raw_buffer_store_b128((v4u){tag, e.x, tag, e.y}, rws, hro, so_hr, AUX_SC1);
    }
    SB();
    STAMP(14);
    unsigned ob[8];
#pragma unroll
    for (int m = 0; m < 8; ++m) ob[m] = pk2(hv[2 * m] + bflo(hn[m]), hv[2 * m + 1] + bfhi(hn[m]));
#pragma unroll
    for (int m = 0; m < 2; ++m) {
      auto r = __builtin_amdgcn_permlane16_swap(ob[m], ob[2 + m], false, false);
      auto q = __builtin_amdgcn_permlane16_swap(ob[4 + m], ob[6 + m], false, false);
      const unsigned r0 = r[0], r1 = r[1], q0 = q[0], q1 = q[1];
      out0[m] = r0; out0[2 + m] = r1; out1[m] = q0; out1[2 + m] = q1;
    }
    PIN(out0); PIN(out1);
  }
  v4u wd[11];
  if (f2 >= 0) {
    const unsigned wa2 = sb + L_FFW + (32 * w + 8 * G + q4) * 32 + p4 * 8;
#pragma unroll
    for (int i = 0; i < 11; ++i) wd[i] = tr16x2(wa2 + ((i == 10 && w == 3) ? 0 : i * 4096), 128);
    if (w == 3) wd[10] = (v4u){0u, 0u, 0u, 0u};
#pragma unroll
    for (int i = 0; i < 11; ++i) PIN(wd[i]);
  }
  RBAR();
  const v2u kk = LDS_U2(sb + L_SEL);
  const unsigned K1 = __builtin_amdgcn_readfirstlane(kk.x), K2 = __builtin_amdgcn_readfirstlane(kk.y);
  STAMP(15);
  if (key != K1 && key != K2) {
    const int col = 64 * gg + 16 * (G & 1) + 8 * (G >> 1);
    __builtin_amdgcn_raw_buffer_store_b128(out0, rout, (unsigned)(tok * 1024 + col * 2), 0, AUX_SC1);
    __builtin_amdgcn_raw_buffer_store_b128(out1, rout, (unsigned)(tok * 1024 + col * 2 + 64), 0, AUX_SC1);
  }
  if (f2 >= 0) {
    const unsigned t1 = 2047u - (K1 & 2047u), t2 = 2047u - (K2 & 2047u);
    const unsigned r1 = WS_HROWS + (t1 >> 4) * 8192, r2 = WS_HROWS + (t2 >> 4) * 8192 + ((t1 >> 4) == (t2 >> 4) ? 4096 : 0);
    v2u hg = {0u, 0u};
    float sg = 0.f;
    if (w == 0) {
      for (unsigned s = 0; s < SPIN_CAP; ++s) {
        asm volatile("" ::: "memory");
        hg = __builtin_amdgcn_raw_buffer_load_b64(rws, ((G & 1) ? r2 : r1) + (16 * f2 + r16) * 8, 0, AUX_SC1);
        if (__all(hg.x == tag)) break;
      }
      const unsigned ordk = ((G & 1) ? K2 : K1) >> 11;
      const unsigned rb = (ordk & 0x8000u) ? (ordk & 0x7fffu) : (~ordk & 0xffffu);
      sg = rbf(__builtin_amdgcn_rcpf(1.f + __builtin_amdgcn_exp2f(bflo(rb) * -1.4426950408889634f)));
      PIN(sg);
    }
    const int ksC = w + 4 * (G + 8);
    unsigned oc = (ksC < 43 ? ksC : w) * 512 + q4 * 128 + p4 * 16, oab = (w + 4 * G) * 512 + q4 * 128 + p4 * 16;
    unsigned a_b = wpriv + G * 32 + (lane & 1) * 16, a_rw = sb + L_PRIV + 8192 + (w * 2 + r16) * 64 + G * 16, a_rr = sb + L_PRIV + 8192 + (G & 1) * 64 + r16 * 4;
    unsigned oout = ((G & 1) ? t2 : t1) * 1024 + (16 * f2 + r16) * 2;
    PIN(oc); PIN(oab); PIN(a_b); PIN(a_rw); PIN(a_rr); PIN(oout);
    v4u ga, gb, gc;
    __builtin_amdgcn_s_sleep(ASLEEP);
    for (unsigned s = 0;; ++s) {
      asm volatile("" ::: "memory");
      ga = __builtin_amdgcn_raw_buffer_load_b128(rws, oab, WS_AGRAN, AUX_SC1);
      gb = __builtin_amdgcn_raw_buffer_load_b128(rws, oab, WS_AGRAN + 8192, AUX_SC1);
      gc = __builtin_amdgcn_raw_buffer_load_b128(rws, oc, WS_AGRAN, AUX_SC1);
      if (__all(ga.x == tag && ga.z == tag && gb.x == tag && gb.z == tag && gc.x == tag && gc.z == tag) || s >= SPIN_CAP) break;
      __builtin_amdgcn_s_sleep(1);
    }
    STAMP(16);
    LDS_U2(a_cr) = (v2u){ga.y, ga.w};
    LDS_U2(a_cr + 512) = (v2u){gb.y, gb.w};
    LDS_U2(a_cr + 1024) = (v2u){gc.y, gc.w};
    WAIT_LGKM();
    v4u bb[11];
#pragma unroll
    for (int i = 0; i < 11; ++i) bb[i] = LDS_U4(a_b + i * 128);
    SB();
    f32x4 d0 = {0.f, 0.f, 0.f, 0.f}, d1 = d0;
#pragma unroll
    for (int i = 0; i < 11; ++i) { if (i & 1) d1 = mfma16(wd[i], bb[i], d1); else d0 = mfma16(wd[i], bb[i], d0); }
    d0 += d1;
    if (r16 < 2) LDS_F4(a_rw) = d0;
    BAR();
    if (w == 0) {
      float y = (LDS_F1(a_rr) + LDS_F1(a_rr + 128)) + (LDS_F1(a_rr + 256) + LDS_F1(a_rr + 384));
      y = rbf(rbf(y) * sg);
      const float t = rbf(bflo(hg.y) + y);
      const unsigned ov = pk2(bfhi(hg.y) + t, 0.f) & 0xffffu;
      const unsigned nb = dppu<0xB1>(ov);
      if (lane < 32 && (lane & 1) == 0) __builtin_amdgcn_raw_buffer_store_b32(ov | (nb << 16), rout, oout, 0, AUX_SC1);
    }
    STAMP(17);
  }
  STAMP(18);
  STAMP_OUT();
  asm volatile(".Lfb_code1:");
}
}

extern "C" void kernel_launch(void* const* d_in, const int* in_sizes, int n_in, void* d_out, int out_size, void* d_ws,
                              size_t ws_size, hipStream_t stream) {
  Params p;
  p.x = (const char*)d_in[0]; p.wqkv = (const char*)d_in[1]; p.wo = (const char*)d_in[2];
  p.wr = (const char*)d_in[4]; p.wg = (const char*)d_in[5]; p.wu = (const char*)d_in[6]; p.wd = (const char*)d_in[7];
  p.out = (char*)d_out; p.ws = (char*)d_ws;
  (void)hipFuncSetAttribute((const void*)fused_block_kernel, hipFuncAttributeMaxDynamicSharedMemorySize, L_TOTAL);
  hipLaunchKernelGGL(fused_block_kernel, dim3(256), dim3(512), L_TOTAL, stream, p);
}
```

```cpp
#include <hip/hip_runtime.h>
#include <hip/hip_bf16.h>

typedef unsigned long long u64;
typedef unsigned v4u __attribute__((ext_vector_type(4)));
typedef unsigned v2u __attribute__((ext_vector_type(2)));
typedef float f32x2 __attribute__((ext_vector_type(2)));
typedef float f32x4 __attribute__((ext_vector_type(4)));
typedef float f32x16 __attribute__((ext_vector_type(16)));
typedef __bf16 bf16x2 __attribute__((ext_vector_type(2)));
typedef __bf16 bf16x4 __attribute__((ext_vector_type(4)));
typedef __bf16 bf16x8 __attribute__((ext_vector_type(8)));
#define GAS __attribute__((address_space(1)))
#define LAS __attribute__((address_space(3)))
#define DI __device__ __forceinline__
#define RLX_AGENT __ATOMIC_RELAXED, __HIP_MEMORY_SCOPE_AGENT

#define STAMPS 0

namespace {
constexpr int FF = 1376;
constexpr int AUX_SC1 = 16;
constexpr unsigned SPIN_CAP = 1u << 16;
constexpr unsigned WS_EPOCH = 0;
constexpr unsigned WS_KVFLAG = 4096;
constexpr unsigned WS_OFLAG = WS_KVFLAG + 256 * 128;
constexpr unsigned WS_KEYS = WS_OFLAG + 512 * 128;
constexpr unsigned WS_AGRAN = WS_KEYS + 256 * 128;
constexpr unsigned WS_STATS = 1u << 18;
constexpr unsigned WS_STAMP = 1u << 19;
constexpr unsigned WS_HROWS = 1u << 20;
constexpr unsigned WS_O = 2u << 20;
constexpr unsigned WS_KV = 4u << 20;
constexpr unsigned WS_BYTES = 8u << 20;
static_assert(WS_AGRAN + 192 * 128 <= WS_STATS && WS_STATS + 128 * 2048 <= WS_STAMP, "ws map");
constexpr int L_X = 0;
constexpr int L_RING = 65536;
constexpr int L_MISC = L_RING + 5 * 16384;
constexpr int L_TAB = L_MISC;
constexpr int L_XS = L_TAB + 1024;
constexpr int L_CS = L_XS + 1024;
constexpr int L_RED = L_CS + 1536;
constexpr int L_QX = L_MISC + 4096;
constexpr int L_WR = L_QX + 8192;
constexpr int L_JUNK = L_WR + 1024;
constexpr int L_SEL = L_RED + 512 - 16;
constexpr int L_FLG = L_RED;
constexpr int L_KEY = L_RED + 64;
constexpr int L_KVX = L_RING + 2 * 16384;
constexpr int L_FFW = 0;
constexpr int L_PRIV = 49152;
constexpr int L_TOTAL = 163840;
static_assert(L_JUNK + 1024 <= L_TOTAL, "lds map");

#define THR1 2
#define THR2 3
#define OSLEEP 2
#define ASLEEP 24
#define GSLEEP 8
struct Params {
  const char* x; const char* wqkv; const char* wo; const char* wr; const char* wg; const char* wu; const char* wd;
  char* out; char* ws;
};

DI float asf(unsigned u) { return __builtin_bit_cast(float, u); }
DI unsigned asu(float f) { return __builtin_bit_cast(unsigned, f); }
DI float bflo(unsigned u) { return asf(u << 16); }
DI float bfhi(unsigned u) { return asf(u & 0xffff0000u); }
DI unsigned pk2(float a, float b) { f32x2 f = {a, b}; return __builtin_bit_cast(unsigned, __builtin_convertvector(f, bf16x2)); }
DI float rbf(float a) { return bflo(pk2(a, a)); }
DI float dot2(unsigned a, unsigned b, float c) {
  return __builtin_amdgcn_fdot2_f32_bf16(__builtin_bit_cast(bf16x2, a), __builtin_bit_cast(bf16x2, b), c, false);
}
DI f32x16 mfma32(v4u a, v4u b, f32x16 c) {
  return __builtin_amdgcn_mfma_f32_32x32x16_bf16(__builtin_bit_cast(bf16x8, a), __builtin_bit_cast(bf16x8, b), c, 0, 0, 0);
}
DI f32x4 mfma16(v4u a, v4u b, f32x4 c) {
  return __builtin_amdgcn_mfma_f32_16x16x32_bf16(__builtin_bit_cast(bf16x8, a), __builtin_bit_cast(bf16x8, b), c, 0, 0, 0);
}
DI float sum32(float x) {
  auto r = __builtin_amdgcn_permlane32_swap(asu(x), asu(x), false, false);
  const unsigned r0 = r[0], r1 = r[1];
  return asf(r0) + asf(r1);
}
DI float sum16(float x) {
  auto r = __builtin_amdgcn_permlane16_swap(asu(x), asu(x), false, false);
  const unsigned r0 = r[0], r1 = r[1];
  return asf(r0) + asf(r1);
}
DI float fmax32(float x) {
  auto r = __builtin_amdgcn_permlane32_swap(asu(x), asu(x), false, false);
  const unsigned r0 = r[0], r1 = r[1];
  return fmaxf(asf(r0), asf(r1));
}
template <int C> DI unsigned dppu(unsigned k) { return (unsigned)__builtin_amdgcn_update_dpp(0, (int)k, C, 0xF, 0xF, true); }
template <int C> DI float dppf(float k) { return asf(dppu<C>(asu(k))); }
DI unsigned umax_row16(unsigned k) {
  k = max(k, dppu<0xB1>(k)); k = max(k, dppu<0x4E>(k)); k = max(k, dppu<0x141>(k)); k = max(k, dppu<0x140>(k));
  return k;
}
DI unsigned umax_wave(unsigned k) {
  k = umax_row16(k);
  { auto r = __builtin_amdgcn_permlane16_swap(k, k, false, false); const unsigned r0 = r[0], r1 = r[1]; k = max(r0, r1); }
  { auto r = __builtin_amdgcn_permlane32_swap(k, k, false, false); const unsigned r0 = r[0], r1 = r[1]; k = max(r0, r1); }
  return k;
}
DI float fsum8(float v) { v += dppf<0xB1>(v); v += dppf<0x4E>(v); v += dppf<0x141>(v); return v; }
DI float fsum_row16(float v) { v = fsum8(v); v += dppf<0x140>(v); return v; }
DI void ln_scalars(float s1, float s2, float& mb, float& rs) {
  const float m = s1 * (1.f / 512.f);
  const float var = fmaxf(fmaf(-m, m, s2 * (1.f / 512.f)), 0.f);
  mb = rbf(m);
  rs = __builtin_amdgcn_rcpf(rbf(__builtin_amdgcn_sqrtf(rbf(rbf(var) + 1e-5f))));
}
DI void glds_chunk(unsigned v0, unsigned v1, const char* ba, const char* bb, unsigned m0v) {
  asm volatile("s_mov_b32 m0, %4\n\ts_nop 0\n\tglobal_load_lds_dwordx4 %0, %2\n\t"
               "s_add_u32 m0, m0, 0x1000\n\ts_nop 0\n\tglobal_load_lds_dwordx4 %1, %2\n\t"
               "s_add_u32 m0, m0, 0x1000\n\ts_nop 0\n\tglobal_load_lds_dwordx4 %0, %3\n\t"
               "s_add_u32 m0, m0, 0x1000\n\ts_nop 0\n\tglobal_load_lds_dwordx4 %1, %3"
               :: "v"(v0), "v"(v1), "s"(ba), "s"(bb), "s"(m0v) : "memory", "scc");
}
DI void glds2(unsigned v, const char* ba, const char* bb, unsigned m0v) {
  asm volatile("s_mov_b32 m0, %3\n\ts_nop 0\n\tglobal_load_lds_dwordx4 %0, %1\n\t"
               "s_add_u32 m0, m0, 0x1000\n\ts_nop 0\n\tglobal_load_lds_dwordx4 %0, %2"
               :: "v"(v), "s"(ba), "s"(bb), "s"(m0v) : "memory", "scc");
}
template <int N> DI void wait_vm() { asm volatile("s_waitcnt vmcnt(%0)" :: "n"(N) : "memory"); }
DI void glds1(unsigned v, const char* ba, unsigned m0v) {
  asm volatile("s_mov_b32 m0, %2\n\ts_nop 0\n\tglobal_load_lds_dwordx4 %0, %1" :: "v"(v), "s"(ba), "s"(m0v) : "memory");
}
#define WAIT_VM(n) asm volatile("s_waitcnt vmcnt(" #n ")" ::: "memory")
#define WAIT_LGKM() asm volatile("s_waitcnt lgkmcnt(0)" ::: "memory")
#define BAR() do { __builtin_amdgcn_sched_barrier(0); asm volatile("s_waitcnt lgkmcnt(0)\n\ts_barrier" ::: "memory"); __builtin_amdgcn_sched_barrier(0); } while (0)
#define PIN(x) asm volatile("" : "+v"(x))
#define SPIN_(x) asm volatile("" : "+s"(x))
#define SB() __builtin_amdgcn_sched_barrier(0)
#define LDS_U4(off) (*(LAS v4u*)(size_t)(off))
#define LDS_F4(off) (*(LAS f32x4*)(size_t)(off))
#define LDS_U2(off) (*(LAS v2u*)(size_t)(off))
#define LDS_F1(off) (*(LAS float*)(size_t)(off))
#define LDS_U1(off) (*(LAS unsigned*)(size_t)(off))
DI v2u tr16(unsigned off) { return __builtin_bit_cast(v2u, __builtin_amdgcn_ds_read_tr16_b64_v4bf16((LAS bf16x4*)(size_t)off)); }
DI v4u tr16x2(unsigned off, unsigned d) { const v2u a = tr16(off), b = tr16(off + d); return (v4u){a.x, a.y, b.x, b.y}; }
DI unsigned read_tag(const char* ws) {
  unsigned e;
  asm volatile("s_load_dword %0, %1, 0x0\n\ts_waitcnt lgkmcnt(0)" : "=s"(e) : "s"(ws) : "memory");
  return e + 1u;
}
DI v4u pack8(const float* o) { return (v4u){pk2(o[0], o[1]), pk2(o[2], o[3]), pk2(o[4], o[5]), pk2(o[6], o[7])}; }

#define STAMP(i) do {} while (0)
#define STAMP_OUT() do {} while (0)

constexpr unsigned ONE2 = 0x3F803F80u;
struct Frags { v4u x[4], a[4], b[4]; };
constexpr int npieces(int n) { return n < 8 ? 6 : (n < 12 ? 4 : 0); }
constexpr int younger(int j) { int s = 0; for (int n = j + 1; n <= j + 3; ++n) s += npieces(n); return s; }

DI void lds_arrive(unsigned addr, int lane) {
  if (lane == 0) __hip_atomic_fetch_add((LAS unsigned*)(size_t)addr, 1u, __ATOMIC_RELAXED, __HIP_MEMORY_SCOPE_WORKGROUP);
}
DI void lds_wait4(unsigned addr) {
  for (unsigned s = 0; s < (SPIN_CAP << 4); ++s) {
    const unsigned v = __hip_atomic_load((LAS unsigned*)(size_t)addr, __ATOMIC_RELAXED, __HIP_MEMORY_SCOPE_WORKGROUP);
    if (__builtin_amdgcn_readfirstlane(v) >= 4u) break;
    __builtin_amdgcn_s_sleep(3);
  }
  asm volatile("" ::: "memory");
}
#define RBAR() asm volatile("s_barrier" ::: "memory")

__global__ __launch_bounds__(512, 2) void fused_block_kernel(Params p) {
  extern __shared__ __attribute__((aligned(16))) char smem[];
  const int lane = threadIdx.x & 63;
  const int wv = __builtin_amdgcn_readfirstlane(threadIdx.x >> 6);
  const int w = wv & 3, tid = threadIdx.x & 255;
  const int b = blockIdx.x, xcd = b & 7, slot = b >> 3;
  const int tt = (xcd & 3) * 8 + (slot & 7), gg = (xcd >> 2) * 4 + (slot >> 3);
  const unsigned sb = (unsigned)(size_t)(LAS char*)smem;
  const int rot = (tt + 2 * gg) & 7;
  const int f1 = slot < 22 ? xcd * 22 + slot : 999;
  const int f2 = (slot >= 22 && slot < 26) ? xcd * 4 + slot - 22 : -1;
  const int r32 = lane & 31, h = lane >> 5, r16 = lane & 15, G = lane >> 4, q4 = r16 >> 2, p4 = lane & 3;
  const int wrow = tid >> 3;
  const unsigned myblk = (unsigned)(tt * 8 + gg);
  const unsigned ov0 = (unsigned)(wrow * 1024 + gg * 128 + (((tid & 7) ^ (((wrow >> 1) & 3) << 1)) << 4)), ov1 = ov0 + 32 * 1024;
  auto issue_wo = [&](int qn) {
    const char* s0 = p.wo + (size_t)(qn * 128) * 1024;
    glds_chunk(ov0, ov1, s0, s0 + 64 * 1024, sb + L_RING + w * 1024 + ((qn + 3) % 5) * 16384);
  };

  if (__builtin_expect(wv < 4, 1)) {
    {
      const unsigned xv = (unsigned)(wrow * 1024 + (((tid & 7) ^ ((wrow >> 1) & 7)) << 4));
      const unsigned wv0 = (unsigned)(wrow * 3072 + gg * 128 + (((tid & 7) ^ (((wrow >> 1) & 1) << 2)) << 4)), wv1 = wv0 + 32 * 3072;
      const unsigned ringw = sb + L_RING + w * 1024;
      const char* xb = p.x + (size_t)tt * 65536;
      auto issue = [&](int n) {
        if (n < 8) {
          const int c = (n + rot) & 7;
          glds2(xv, xb + c * 128, xb + c * 128 + 32768, sb + L_X + n * 8192 + w * 1024);
          const char* s0 = p.wqkv + (size_t)(c * 64) * 3072 + 1024;
          glds_chunk(wv0, wv1, s0, s0 + 1024, ringw + (n % 5) * 16384);
        } else if (n < 12) {
          glds_chunk(wv0, wv1, p.wqkv + (size_t)(((2 * n + rot) & 7) * 64) * 3072, p.wqkv + (size_t)(((2 * n + 1 + rot) & 7) * 64) * 3072,
                     ringw + (n % 5) * 16384);
        }
      };
      asm volatile(".Lfb_codeA:");
      STAMP(0);
#pragma unroll
      for (int n = 0; n < 4; ++n) issue(n);
      STAMP(19);
#define LSTEP(j) do { wait_vm<younger(j)>(); RBAR(); issue((j) + 4); } while (0)
      LSTEP(0); LSTEP(1); LSTEP(2); LSTEP(3); LSTEP(4); LSTEP(5); LSTEP(6); LSTEP(7);
      const int u = b * 4 + w, u2 = 1024 + (u % 352);
      glds1(ov0, p.wo + (size_t)(tt & 7) * 65536, sb + L_JUNK);
      glds1(ov1, p.wo + (size_t)(tt & 7) * 65536, sb + L_JUNK);
      glds1((unsigned)lane * 16, p.wg + (size_t)u * 1024, sb + L_JUNK);
      glds1((unsigned)lane * 16, p.wu + (size_t)u * 1024, sb + L_JUNK);
      glds1((unsigned)lane * 16, p.wd + (size_t)u * 1024, sb + L_JUNK);
      glds1((unsigned)lane * 16, (u < 352 ? p.wg : u < 704 ? p.wu : p.wd) + (size_t)u2 * 1024, sb + L_JUNK);
      glds1((unsigned)lane * 16, p.wd + (size_t)(1024 + 320 + (u & 31)) * 1024, sb + L_JUNK);
    }
    const unsigned tag = read_tag(p.ws);
    const __amdgpu_buffer_rsrc_t rws = __builtin_amdgcn_make_buffer_rsrc(p.ws, 0, WS_BYTES, 0x00020000);
    RBAR();
    unsigned so_ow = WS_O + (tt * 64 + gg * 2 + (w >> 1)) * 1024, so_ofw = WS_OFLAG + (tt * 16 + gg) * 128, so_kvf = WS_KVFLAG + myblk * 128;
    unsigned sot[4];
#pragma unroll
    for (int t = 0; t < 4; ++t) { sot[t] = WS_KV + (tt - 4 + t >= 0 ? myblk - (4 - t) * 8 : myblk) * 16384; SPIN_(sot[t]); }
    SPIN_(so_ow); SPIN_(so_ofw); SPIN_(so_kvf);
    unsigned kvfo;
    {
      const int st = tt - 4 + (lane & 3);
      kvfo = WS_KVFLAG + ((st < 0 ? tt : st) * 8 + gg) * 128;
    }
    unsigned lo = w * 1024 + lane * 16, lo1 = lo + 4096, lo2 = lo + 8192, lo3 = lo + 12288;
    unsigned a_q = sb + L_QX + lo, a_kv = sb + L_KVX + lo;
    unsigned ooff0 = ((unsigned)(r32 >> 4) * 16) * 1024 + (r16 + 16 * ((w & 1) * 2 + h)) * 16, ooff1 = ooff0 + 32768;
    PIN(kvfo); PIN(lo); PIN(lo1); PIN(lo2); PIN(lo3); PIN(a_q); PIN(a_kv); PIN(ooff0); PIN(ooff1);
    wait_vm<15>(); RBAR();
    unsigned pm[8];
#pragma unroll
    for (int i = 0; i < 8; ++i) {
      const int k0 = ((2 * i) & 3) + 8 * (i >> 1) + 4 * h;
      pm[i] = (k0 <= r32 ? 0xffffu : 0u) | (k0 + 1 <= r32 ? 0xffff0000u : 0u);
      PIN(pm[i]);
    }
    u64 vis[16];
#pragma unroll
    for (int i = 0; i < 16; ++i) {
      const int c0 = (i & 3) + 8 * (i >> 2);
      vis[i] = (u64)(0xffffffffu << c0) | ((u64)(0xffffffffu << (c0 + 4)) << 32);
      SPIN_(vis[i]);
    }
    auto sel = [&](int i, float yes, float no) {
      float r;
      asm("v_cndmask_b32_e64 %0, %1, %2, %3" : "=v"(r) : "v"(no), "v"(yes), "s"(vis[i]));
      return r;
    };
    const int first = 8 - 2 * tt;
    f32x16 cz;
    auto fill = [&](f32x16& blk, float v) {
      f32x2 v2 = {v, v};
      PIN(v2);
#pragma unroll
      for (int i = 0; i < 16; i += 2) { blk[i] = v2[0]; blk[i + 1] = v2[1]; }
      PIN(blk);
    };
    fill(cz, first > 0 ? -1e30f : 0.f);
    v4u kf[10], va[10], vb[10], qf[2];
#pragma unroll
    for (int k = 0; k < 10; ++k) { vb[k] = (v4u){ONE2, ONE2, ONE2, ONE2}; PIN(vb[k]); }
    wait_vm<7>(); RBAR();
    if (threadIdx.x == 0) __builtin_amdgcn_raw_buffer_store_b32(tag, rws, 0, so_kvf, AUX_SC1);
    issue_wo(0); issue_wo(1);
    STAMP(3);
    RBAR();
    unsigned fl = __builtin_amdgcn_raw_buffer_load_b32(rws, kvfo, 0, AUX_SC1);
    kf[8] = LDS_U4(a_kv); kf[9] = LDS_U4(a_kv + 8192);
    va[8] = LDS_U4(a_kv + 4096); va[9] = LDS_U4(a_kv + 12288);
    auto vfix = [&](int k) {
#pragma unroll
      for (int j = 0; j < 4; ++j) {
        auto r = __builtin_amdgcn_permlane16_swap(va[k][j], vb[k][j], false, false);
        const unsigned r0 = r[0], r1 = r[1];
        va[k][j] = r0; vb[k][j] = r1;
      }
      PIN(va[k]); PIN(vb[k]);
    };
    vfix(8); vfix(9);
    SB();
    for (unsigned s = 0; s < SPIN_CAP; ++s) {
      if (__all(fl == tag)) break;
      __builtin_amdgcn_s_sleep(1);
      asm volatile("" ::: "memory");
      fl = __builtin_amdgcn_raw_buffer_load_b32(rws, kvfo, 0, AUX_SC1);
    }
    asm volatile("" ::: "memory");
    STAMP(4);
#pragma unroll
    for (int k = 0; k < 8; ++k) kf[k] = __builtin_amdgcn_raw_buffer_load_b128(rws, (k & 1) ? lo2 : lo, sot[k >> 1], AUX_SC1);
    SB();
    RBAR();
    qf[0] = LDS_U4(a_q); qf[1] = LDS_U4(a_q + 4096);
#pragma unroll
    for (int k = 0; k < 8; ++k) va[k] = __builtin_amdgcn_raw_buffer_load_b128(rws, (k & 1) ? lo3 : lo1, sot[k >> 1], AUX_SC1);
    SB();
    RBAR();
    auto oflag = [&](int qb) {
      WAIT_VM(0);
      if (lane == 0) {
        const unsigned n = __hip_atomic_fetch_add((LAS unsigned*)(size_t)(sb + L_FLG + 8 + qb * 4), 1u, __ATOMIC_RELAXED, __HIP_MEMORY_SCOPE_WORKGROUP);
        if (n == 3u) __builtin_amdgcn_raw_buffer_store_b32(tag, rws, qb * 1024, so_ofw, AUX_SC1);
      }
    };
    STAMP(5);
    f32x16 zero;
#pragma unroll
    for (int i = 0; i < 16; ++i) zero[i] = 0.f;
    float ma[2], mb[2];
    auto mx16 = [&](int qb, const f32x16& s) {
#pragma unroll
      for (int i = 0; i < 16; i += 4) { ma[qb] = fmaxf(fmaxf(ma[qb], s[i]), s[i + 1]); mb[qb] = fmaxf(fmaxf(mb[qb], s[i + 2]), s[i + 3]); }
      PIN(ma[qb]); PIN(mb[qb]);
    };
    ma[0] = mb[0] = ma[1] = mb[1] = -3e38f;
    {
      f32x16 sm[2];
      sm[0] = mfma32(kf[8], qf[0], zero); PIN(sm[0]);
      f32x16 s0 = mfma32(kf[0], qf[0], cz); PIN(s0);
      sm[1] = mfma32(kf[9], qf[1], zero); PIN(sm[1]);
      f32x16 s1 = mfma32(kf[1], qf[1], cz); PIN(s1);
      asm volatile("s_nop 7\n\ts_nop 1");
      SB();
#pragma unroll
      for (int i = 0; i < 16; ++i) sm[0][i] = sel(i, sm[0][i], s0[i]);
      SB();
      s0 = mfma32(kf[1], qf[0], cz); PIN(s0);
      SB();
#pragma unroll
      for (int i = 0; i < 16; ++i) sm[1][i] = sel(i, sm[1][i], s1[i]);
      SB();
      s1 = mfma32(kf[8], qf[1], zero); PIN(s1);
      SB();
      mx16(0, sm[0]); mx16(1, sm[1]);
      SB();
#pragma unroll
      for (int k = 2; k < 8; ++k) {
        if ((k & 1) == 0 && __builtin_expect(first == k, 0)) fill(cz, 0.f);
        mx16(0, s0); SB();
        s0 = mfma32(kf[k], qf[0], cz); PIN(s0);
        SB();
        mx16(1, s1);
        SB();
        s1 = mfma32(kf[k], qf[1], cz); PIN(s1);
        vfix(k - 2);
        SB();
      }
      mx16(0, s0);
      vfix(6);
      SB();
      mx16(1, s1);
      vfix(7);
      STAMP(6);
      float m0, m1;
      {
        auto r = __builtin_amdgcn_permlane32_swap(asu(fmaxf(ma[0], mb[0])), asu(fmaxf(ma[1], mb[1])), false, false);
        const unsigned r0 = r[0], r1 = r[1];
        const float mm = fmaxf(asf(r0), asf(r1));
        auto q = __builtin_amdgcn_permlane32_swap(asu(mm), asu(mm), false, false);
        const unsigned q0 = q[0], q1 = q[1];
        m0 = asf(q0); m1 = asf(q1);
      }
#pragma unroll
      for (int qb = 0; qb < 2; ++qb) {
        const float mx = qb ? m1 : m0;
        f32x16 o;
        v4u pp;
        fill(cz, first > 1 + qb ? -1e30f : -mx);
        SB();
        s0 = mfma32(kf[1 + qb], qf[qb], cz);
        PIN(s0);
        SB();
        {
          float e[16];
#pragma unroll
          for (int i = 0; i < 16; ++i) e[i] = __builtin_amdgcn_exp2f(sm[qb][i] - mx);
          const v4u p0 = pack8(e), p1 = pack8(e + 8);
          v4u d0, d1, f0, f1_;
#pragma unroll
          for (int j = 0; j < 4; ++j) { d0[j] = p0[j] & pm[j]; f0[j] = p0[j] ^ d0[j]; d1[j] = p1[j] & pm[4 + j]; f1_[j] = p1[j] ^ d1[j]; }
          o = mfma32(va[8 + qb], d0, zero);
          o = mfma32(va[qb], f0, o);
          o = mfma32(vb[8 + qb], d1, o);
          pp = f1_;
          PIN(pp); PIN(o);
        }
        SB();
#pragma unroll
        for (int k = 1 + qb; k < 8 + qb; ++k) {
          float e[16];
          if (k + 1 < 8 + qb && (k & 1) == 1 && __builtin_expect(first == k + 1, 0)) fill(cz, -mx);
#pragma unroll
          for (int i = 0; i < 4; ++i) e[i] = __builtin_amdgcn_exp2f(s0[i]);
          SB();
          o = mfma32(vb[k - 1], pp, o);
          PIN(o);
          SB();
#pragma unroll
          for (int i = 4; i < 8; ++i) e[i] = __builtin_amdgcn_exp2f(s0[i]);
          SB();
          if (k + 1 < 8 + qb) { s1 = mfma32(kf[k + 1], qf[qb], cz); PIN(s1); }
          SB();
          v4u p0 = pack8(e);
          PIN(p0);
          SB();
          e[8] = __builtin_amdgcn_exp2f(s0[8]); e[9] = __builtin_amdgcn_exp2f(s0[9]);
          SB();
          o = mfma32(va[k], p0, o);
          PIN(o);
          SB();
#pragma unroll
          for (int i = 10; i < 16; ++i) e[i] = __builtin_amdgcn_exp2f(s0[i]);
          pp = pack8(e + 8);
          PIN(pp);
          if (qb == 1 && k == 5) oflag(0);
          SB();
          s0 = s1;
        }
        o = mfma32(vb[7 + qb], pp, o);
        const float inv = __builtin_amdgcn_rcpf(o[8]);
        float on[8];
#pragma unroll
        for (int i = 0; i < 8; ++i) on[i] = o[i] * inv;
        __builtin_amdgcn_raw_buffer_store_b128(pack8(on), rws, qb ? ooff1 : ooff0, so_ow, AUX_SC1);
      }
    }
    STAMP(7);
    oflag(1);
    STAMP(8);
    if (w != 0) { STAMP_OUT(); return; }
    v4u a[16];
    if (f1 < 172) {
      lds_wait4(sb + L_FLG + 4);
      const unsigned wa = sb + L_FFW + (p4 >> 1) * 8192 + (8 * G + q4) * 16 + (p4 & 1) * 8;
#pragma unroll
      for (int i = 0; i < 16; ++i) a[i] = tr16x2(wa + i * 512, 64);
#pragma unroll
      for (int i = 0; i < 16; ++i) PIN(a[i]);
    }
    unsigned so_keyr = WS_KEYS + xcd * 4096, so_key = WS_KEYS + tt * 128, kro = r32 * 128, l16 = lane * 16, st_w = sb + L_PRIV + 8192 + lane * 4, st_r = sb + L_PRIV + 8192 + (lane & 1) * 1024 + G * 16;
    SPIN_(so_keyr); SPIN_(so_key); PIN(kro); PIN(l16); PIN(st_w); PIN(st_r);
    RBAR();
    if (gg == 0) {
      const unsigned kq = LDS_U1(sb + L_KEY + (lane & 7) * 4);
      unsigned k1 = max(kq, dppu<0xB1>(kq)); k1 = max(k1, dppu<0x4E>(k1)); k1 = max(k1, dppu<0x141>(k1));
      unsigned k2 = kq == k1 ? 0u : kq; k2 = max(k2, dppu<0xB1>(k2)); k2 = max(k2, dppu<0x4E>(k2)); k2 = max(k2, dppu<0x141>(k2));
      if (lane < 8) __builtin_amdgcn_raw_buffer_store_b128((v4u){tag, k1, tag, k2}, rws, lane * 4096, so_key, AUX_SC1);
    }
    unsigned K1, K2;
    {
      v4u ka;
      for (unsigned s = 0;; ++s) {
        asm volatile("" ::: "memory");
        ka = __builtin_amdgcn_raw_buffer_load_b128(rws, kro, so_keyr, AUX_SC1);
        if (__all(ka.x == tag && ka.z == tag) || s >= SPIN_CAP) break;
        __builtin_amdgcn_s_sleep(2);
      }
      const unsigned kk = h ? ka.w : ka.y;
      K1 = __builtin_amdgcn_readfirstlane(umax_wave(kk));
      K2 = __builtin_amdgcn_readfirstlane(umax_wave(kk == K1 ? 0u : kk));
    }
    STAMP(15);
    const unsigned g1 = (2047u - (K1 & 2047u)) >> 4, g2 = (2047u - (K2 & 2047u)) >> 4;
    const unsigned r1 = WS_HROWS + g1 * 8192, r2 = WS_HROWS + g2 * 8192 + (g1 == g2 ? 4096 : 0);
    v4u g[8];
    if (f1 < 172) {
#pragma unroll
      for (int i = 0; i < 8; ++i) g[i] = __builtin_amdgcn_raw_buffer_load_b128(rws, l16 + (i & 3) * 1024, i < 4 ? r1 : r2, AUX_SC1);
    }
    if (lane == 0) LDS_U2(sb + L_SEL) = (v2u){K1, K2};
    asm volatile("s_waitcnt lgkmcnt(0)\n\ts_barrier" ::: "memory");
    if (b == 255 && lane == 0) __builtin_amdgcn_raw_buffer_store_b32(tag, rws, 0, WS_EPOCH, AUX_SC1);
    if (f1 < 172) {
      for (unsigned s = 0;; ++s) {
        bool ok = true;
#pragma unroll
        for (int i = 0; i < 8; ++i) ok = ok && g[i].x == tag && g[i].z == tag;
        if (__all(ok) || s >= SPIN_CAP) break;
        asm volatile("" ::: "memory");
#pragma unroll
        for (int i = 0; i < 8; ++i) g[i] = __builtin_amdgcn_raw_buffer_load_b128(rws, l16 + (i & 3) * 1024, i < 4 ? r1 : r2, AUX_SC1);
      }
#pragma unroll
      for (int i = 0; i < 8; ++i) LDS_U1(st_w + i * 256) = __builtin_amdgcn_perm(g[i].w, g[i].y, 0x05040100u);
      WAIT_LGKM();
      v4u bb[16];
#pragma unroll
      for (int i = 0; i < 16; ++i) bb[i] = LDS_U4(st_r + i * 64);
      SB();
      f32x4 d0 = {0.f, 0.f, 0.f, 0.f}, d1 = d0;
#pragma unroll
      for (int i = 0; i < 16; i += 2) { d0 = mfma16(a[i], bb[i], d0); d1 = mfma16(a[i + 1], bb[i + 1], d1); }
      d0 += d1;
      float av[4];
#pragma unroll
      for (int j = 0; j < 4; ++j) {
        auto r = __builtin_amdgcn_permlane32_swap(asu(d0[j]), asu(d0[j]), false, false);
        const unsigned r0 = r[0], r1_ = r[1];
        const float gs = rbf(asf(r0)), us = rbf(asf(r1_));
        av[j] = rbf(gs * __builtin_amdgcn_rcpf(1.f + __builtin_amdgcn_exp2f(gs * -1.4426950408889634f))) * us;
      }
      if (lane < 32 && r16 < 2)
        __builtin_amdgcn_raw_buffer_store_b128((v4u){tag, pk2(av[0], av[1]), tag, pk2(av[2], av[3])}, rws, r16 * 32 + G * 16, WS_AGRAN + f1 * 128, AUX_SC1);
      STAMP(16);
    }
    STAMP_OUT();
    asm volatile(".Lfb_code2:");
    return;
  }

  STAMP(0);
  {
    const char* pc;
    int lo_, hi_;
    asm volatile("s_getpc_b64 %0\n.Lfb_code0:\n\ts_mov_b32 %1, .Lfb_codeA-.Lfb_code0\n\ts_mov_b32 %2, .Lfb_code1-.Lfb_code0\n\t"
                 "s_max_i32 %2, %2, .Lfb_code2-.Lfb_code0\n\ts_min_i32 %1, %1, 0" : "=s"(pc), "=s"(lo_), "=s"(hi_) :: "scc");
    const int off = min((slot * 4 + w) * 1024, hi_ - lo_ - 1024);
    if ((slot * 4 + w) * 1024 < hi_ - lo_) glds1((unsigned)lane * 16, pc + lo_ + off, sb + L_JUNK);
  }
  if (tid < 4) LDS_U1(sb + L_FLG + tid * 4) = 0u;
  const unsigned tag = read_tag(p.ws);
  const __amdgpu_buffer_rsrc_t rws = __builtin_amdgcn_make_buffer_rsrc(p.ws, 0, WS_BYTES, 0x00020000);
  const int tb = w & 1, ct = w >> 1;
  unsigned so_kv = WS_KV + myblk * 16384;
  SPIN_(so_kv);
  unsigned xa[4], wa[4], wb[4];
#pragma unroll
  for (int i = 0; i < 4; ++i) {
    const int ks = i ^ (ct | (tb << 1));
    xa[i] = sb + L_X + (tb * 32 + r32) * 128 + (((2 * ks + h) ^ ((r32 >> 1) & 7)) << 4);
    wa[i] = sb + L_RING + (16 * ks + 8 * h + q4) * 128 + ((ct ^ ((q4 >> 1) & 1)) << 6) + (G & 1) * 32 + p4 * 8;
    wb[i] = wa[i] + 49152;
    PIN(xa[i]); PIN(wa[i]); PIN(wb[i]);
  }
  unsigned a_csw = sb + L_CS + h * 512 + w * 128 + r32 * 4, a_xsw = sb + L_XS + w * 256 + r32 * 8 + h * 4;
  unsigned a_xsr = sb + L_XS + tb * 256 + r32 * 8, a_csr = sb + L_CS + ct * 256 + h * 16, a_cvr = sb + L_CS + 512 + ct * 256 + r32 * 4;
  unsigned a_tabw = sb + L_TAB + w * 256 + lane * 4, a_tabr = sb + L_TAB + w * 256 + h * 16, a_cqw = sb + L_CS + 1024 + w * 128 + r32 * 4;
  unsigned ko = tb * 8192 + 2 * ct * 1024 + lane * 16;
  unsigned vo = tb * 8192 + 4096 + (2 * ct + (r32 >> 4)) * 1024 + (h * 32 + r16) * 16;
  unsigned a_kx = sb + L_KVX + ko, a_vx = sb + L_KVX + vo, a_qw = sb + L_QX + (tb * 4 + 2 * ct) * 1024 + lane * 16;
  unsigned a_xres = sb + L_X + ((gg - rot) & 7) * 8192 + (w * 16 + r16) * 128 + (((G >> 1) ^ ((r16 >> 1) & 7)) << 4) + (G & 1) * 8;
  PIN(a_csw); PIN(a_xsw); PIN(a_xsr); PIN(a_csr); PIN(a_cvr); PIN(a_tabw); PIN(a_tabr); PIN(a_cqw);
  PIN(ko); PIN(vo); PIN(a_kx); PIN(a_vx); PIN(a_qw); PIN(a_xres);
  f32x16 acc0, acc1;
#pragma unroll
  for (int i = 0; i < 16; ++i) { acc0[i] = 0.f; acc1[i] = 0.f; }
  float cs0 = 0.f, cs1 = 0.f, xs = 0.f, xq = 0.f;
  auto rd = [&](Frags& f, int slab, int sl) {
#pragma unroll
    for (int i = 0; i < 4; ++i) {
      const unsigned wo = sl < 3 ? wa[i] + sl * 16384 : wb[i] + (sl - 3) * 16384;
      f.x[i] = LDS_U4(xa[i] + slab * 8192); f.a[i] = tr16x2(wo, 512); f.b[i] = tr16x2(wo + 8192, 512);
    }
  };
  auto sums = [&](const Frags& f, bool withx) {
    PIN(acc0);
    if (withx) PIN(acc1);
#pragma unroll
    for (int i = 0; i < 8; ++i) { __builtin_amdgcn_sched_group_barrier(0x8, 1, 0); __builtin_amdgcn_sched_group_barrier(0x100, 3, 0); }
    SB();
#pragma unroll
    for (int j = 0; j < 4; ++j) {
      cs0 = dot2(f.a[0][j], ONE2, cs0); cs1 = dot2(f.b[0][j], ONE2, cs1);
      if (withx) { xs = dot2(f.x[0][j], ONE2, xs); xq = dot2(f.x[0][j], f.x[0][j], xq); }
    }
#pragma unroll
    for (int j = 0; j < 4; ++j) {
      cs0 = dot2(f.a[1][j], ONE2, cs0); cs1 = dot2(f.b[1][j], ONE2, cs1);
      if (withx) { xs = dot2(f.x[2][j], ONE2, xs); xq = dot2(f.x[2][j], f.x[2][j], xq); }
    }
    PIN(cs0); PIN(cs1);
    if (withx) { PIN(xs); PIN(xq); }
    SB();
  };
  auto mmA = [&](const Frags& f) {
#pragma unroll
    for (int i = 0; i < 4; ++i) { acc0 = mfma32(f.a[i], f.x[i], acc0); acc1 = mfma32(f.x[i], f.b[i], acc1); }
  };
  Frags fa, fb2;
  PIN(acc0); PIN(acc1);
  BAR(); rd(fa, 0, 0); sums(fa, true);
  STAMP(20);
  BAR(); rd(fb2, 1, 1); mmA(fa); sums(fb2, true);
  BAR(); rd(fa, 2, 2); mmA(fb2); sums(fa, true);
  BAR(); rd(fb2, 3, 3); mmA(fa); sums(fb2, true);
  BAR(); rd(fa, 4, 4); mmA(fb2); sums(fa, true);
  BAR(); rd(fb2, 5, 0); mmA(fa); sums(fb2, true);
  BAR(); rd(fa, 6, 1); mmA(fb2); sums(fa, true);
  BAR(); rd(fb2, 7, 2); mmA(fa); sums(fb2, true);
  {
    auto rc = __builtin_amdgcn_permlane32_swap(asu(cs0), asu(cs1), false, false);
    auto rx = __builtin_amdgcn_permlane32_swap(asu(xs), asu(xq), false, false);
    const unsigned c0 = rc[0], c1 = rc[1], x0 = rx[0], x1 = rx[1];
    LDS_F1(a_csw) = asf(c0) + asf(c1);
    LDS_F1(a_xsw) = asf(x0) + asf(x1);
  }
  cs0 = 0.f; cs1 = 0.f;
  STAMP(27);
  BAR();
  float rs1, mr1;
  v4u xh[4];
  auto rdx = [&](int slab) {
#pragma unroll
    for (int i = 0; i < 4; ++i) xh[i] = LDS_U4(xa[i] + slab * 8192);
  };
  {
    const v2u t0 = LDS_U2(a_xsr), t1 = LDS_U2(a_xsr + 512);
    f32x4 ca[4], cb[4];
#pragma unroll
    for (int g = 0; g < 4; ++g) { ca[g] = LDS_F4(a_csr + g * 32); cb[g] = LDS_F4(a_csr + 128 + g * 32); }
    const float cv0 = LDS_F1(a_cvr), cv1 = LDS_F1(a_cvr + 128);
    SB();
#pragma unroll
    for (int i = 0; i < 4; ++i) acc0 = mfma32(fb2.a[i], fb2.x[i], acc0);
    PIN(acc0);
    float mb;
    ln_scalars(asf(t0.x) + asf(t1.x), asf(t0.y) + asf(t1.y), mb, rs1);
    mr1 = -mb * rs1;
    LDS_F1(a_tabw) = h ? mr1 : rs1;
    f32x4 r4[4], m4[4];
#pragma unroll
    for (int g = 0; g < 4; ++g) { r4[g] = LDS_F4(a_tabr + g * 32); m4[g] = LDS_F4(a_tabr + 128 + g * 32); }
    SB();
#pragma unroll
    for (int i = 0; i < 4; ++i) acc1 = mfma32(fb2.x[i], fb2.b[i], acc1);
    PIN(acc1);
    float o[16];
#pragma unroll
    for (int g = 0; g < 4; ++g)
#pragma unroll
      for (int j = 0; j < 4; ++j) o[4 * g + j] = fmaf(acc0[4 * g + j], rs1, mr1 * (ca[g][j] + cb[g][j]));
    const v4u k0 = pack8(o), k1 = pack8(o + 8);
#pragma unroll
    for (int i = 0; i < 4; ++i) { __builtin_amdgcn_sched_group_barrier(0x8, 1, 0); __builtin_amdgcn_sched_group_barrier(0x2, 6, 0); }
    SB();
    __builtin_amdgcn_raw_buffer_store_b128(k0, rws, ko, so_kv, AUX_SC1);
    __builtin_amdgcn_raw_buffer_store_b128(k1, rws, ko + 1024, so_kv, AUX_SC1);
    SB();
    const float cv = cv0 + cv1;
#pragma unroll
    for (int g = 0; g < 4; ++g)
#pragma unroll
      for (int j = 0; j < 4; ++j) o[4 * g + j] = fmaf(acc1[4 * g + j], r4[g][j], m4[g][j] * cv);
    const v4u v0 = pack8(o), v1 = pack8(o + 8);
    __builtin_amdgcn_raw_buffer_store_b128(v0, rws, vo, so_kv, AUX_SC1);
    __builtin_amdgcn_raw_buffer_store_b128(v1, rws, vo + 256, so_kv, AUX_SC1);
    SB();
    STAMP(1);
    RBAR();
    rd(fa, 0, 3); rdx(1);
    LDS_U4(a_kx) = k0; LDS_U4(a_kx + 1024) = k1;
    LDS_U4(a_vx) = v0; LDS_U4(a_vx + 256) = v1;
  }
#pragma unroll
  for (int i = 0; i < 16; ++i) acc0[i] = 0.f;
  auto mmB = [&](const Frags& f) {
#pragma unroll
    for (int i = 0; i < 4; ++i) acc0 = mfma32(f.b[i], xh[i], acc0);
#pragma unroll
    for (int i = 0; i < 4; ++i) acc0 = mfma32(f.a[i], f.x[i], acc0);
  };
  sums(fa, false);
  rd(fb2, 2, 4); mmB(fa); rdx(3); sums(fb2, false);
  WAIT_VM(0);
  BAR();
  SB();
  STAMP(2);
  rd(fa, 4, 0); mmB(fb2); rdx(5); sums(fa, false);
  rd(fb2, 6, 1); mmB(fa); rdx(7);
  v2u xres[4];
#pragma unroll
  for (int c = 0; c < 4; ++c) xres[c] = LDS_U2(a_xres ^ (c * 32));
  sums(fb2, false);
  cs0 = sum32(cs0 + cs1);
  LDS_F1(a_cqw) = cs0;
  mmB(fb2);
  PIN(acc0);
  BAR();
  {
    f32x4 ca[4], cb[4];
#pragma unroll
    for (int g = 0; g < 4; ++g) { ca[g] = LDS_F4(a_csr + 1024 + g * 32); cb[g] = LDS_F4(a_csr + 1024 + 128 + g * 32); }
    SB();
    float o[16];
    const float sc = 0.25f * 1.4426950408889634f;
#pragma unroll
    for (int g = 0; g < 4; ++g)
#pragma unroll
      for (int j = 0; j < 4; ++j) o[4 * g + j] = fmaf(acc0[4 * g + j], rs1, mr1 * (ca[g][j] + cb[g][j]));
    const v4u a0 = pack8(o), a1 = pack8(o + 8);
#pragma unroll
    for (int j = 0; j < 4; ++j) { o[2 * j] = bflo(a0[j]) * sc; o[2 * j + 1] = bfhi(a0[j]) * sc; o[8 + 2 * j] = bflo(a1[j]) * sc; o[9 + 2 * j] = bfhi(a1[j]) * sc; }
    LDS_U4(a_qw) = pack8(o);
    LDS_U4(a_qw + 1024) = pack8(o + 8);
  }
  BAR();
  STAMP(3);
  RBAR();
  {
    issue_wo(2); issue_wo(3);
    glds1((unsigned)lane * 16, p.wr, sb + L_WR);
    STAMP(21);
    WAIT_VM(0);
    STAMP(22);
    lds_arrive(sb + L_FLG, lane);
  }
  if (f1 < 172) {
#pragma unroll 1
    for (int i = w * 2; i < w * 2 + 2; ++i) {
      glds1((unsigned)lane * (FF * 2), p.wg + f1 * 16 + (size_t)i * 64 * FF * 2, sb + L_FFW + i * 1024);
      wait_vm<THR1>();
      glds1((unsigned)lane * (FF * 2), p.wu + f1 * 16 + (size_t)i * 64 * FF * 2, sb + L_FFW + 8192 + i * 1024);
      wait_vm<THR1>();
    }
  } else if (f2 >= 0) {
#pragma unroll 1
    for (int i = w; i < 43; i += 4) {
      glds1((unsigned)((lane >> 1) * 1024 + (lane & 1) * 16), p.wd + f2 * 32 + (size_t)i * 32 * 1024, sb + L_FFW + i * 1024);
      wait_vm<THR2>();
    }
  }
  const int tok = tt * 64 + w * 16 + r16;
  unsigned so_ofr = WS_OFLAG + (tt * 2 + (w >> 1)) * 1024;
  unsigned so_st = WS_STATS + (tt * 4 + w) * 2048, so_hr = WS_HROWS + (tt * 4 + w) * 8192 + gg * 512;
  unsigned oq[4];
#pragma unroll
  for (int qn = 0; qn < 4; ++qn) { oq[qn] = WS_O + (tt * 4 + w) * 16384 + qn * 4096; SPIN_(oq[qn]); }
  SPIN_(so_ofr); SPIN_(so_st); SPIN_(so_hr);
  unsigned woa[4];
#pragma unroll
  for (int c = 0; c < 4; ++c)
    woa[c] = sb + L_RING + (16 * (G >> 1) + 4 * (G & 1) + q4) * 128 + ((c ^ (2 * (G & 1) + (q4 >> 1))) << 5) + p4 * 8;
  auto wofrag = [&](int qn, int ks, int c) { return tr16x2(woa[c] + ((qn + 3) % 5) * 16384 + ks * 4096, 1024); };
  lds_wait4(sb + L_FLG);
  STAMP(4);
  v4u wf[2][4][4];
#pragma unroll
  for (int qn = 0; qn < 2; ++qn)
#pragma unroll
    for (int ks = 0; ks < 4; ++ks)
#pragma unroll
      for (int c = 0; c < 4; ++c) wf[qn][ks][c] = wofrag(qn, ks, c);
  v2u wr[4];
#pragma unroll
  for (int c = 0; c < 4; ++c) { wr[c] = LDS_U2(sb + L_WR + gg * 128 + G * 8 + c * 32); PIN(wr[c]); }
  const v4u wrall = LDS_U4(sb + L_WR + lane * 16);
  float wrsum;
  {
    float s = 0.f;
#pragma unroll
    for (int j = 0; j < 4; ++j) s = dot2(wrall[j], ONE2, s);
    wrsum = sum32(sum16(fsum_row16(s)));
    PIN(wrsum);
  }
#pragma unroll
  for (int qn = 0; qn < 2; ++qn)
#pragma unroll
    for (int ks = 0; ks < 4; ++ks)
#pragma unroll
      for (int c = 0; c < 4; ++c) PIN(wf[qn][ks][c]);
  WAIT_VM(0);
  lds_arrive(sb + L_FLG + 4, lane);
  STAMP(5);
  const __amdgpu_buffer_rsrc_t rout = __builtin_amdgcn_make_buffer_rsrc(p.out, 0, 2048 * 1024, 0x00020000);
  const unsigned wpriv = sb + L_PRIV + w * 2048;
  unsigned l16 = lane * 16, ofo = (lane & 7) * 128, sto = lane * 16 + gg * 256;
  unsigned key, a_cw = wpriv + G * 16, a_cr = wpriv + lane * 8, hro = h * 4096 + r32 * 16, keylo = 2047 - tok;
  PIN(l16); PIN(ofo); PIN(sto); PIN(a_cw); PIN(a_cr); PIN(hro); PIN(keylo);
  float hv[16];
  {
    lds_wait4(sb + L_FLG + 8 + (w >> 1) * 4);
    __builtin_amdgcn_s_sleep(OSLEEP);
    for (unsigned s = 0; s < SPIN_CAP; ++s) {
      asm volatile("" ::: "memory");
      const unsigned f = __builtin_amdgcn_raw_buffer_load_b32(rws, ofo, so_ofr, AUX_SC1);
      if (__all(f == tag)) break;
      __builtin_amdgcn_s_sleep(2);
    }
    asm volatile("" ::: "memory");
    STAMP(9);
    v4u of[4][4];
#pragma unroll
    for (int qn = 0; qn < 4; ++qn)
#pragma unroll
      for (int ks = 0; ks < 4; ++ks) of[qn][ks] = __builtin_amdgcn_raw_buffer_load_b128(rws, l16 + ks * 1024, oq[qn], AUX_SC1);
    asm volatile("" ::: "memory");
    SB();
    f32x4 a[4];
#pragma unroll
    for (int c = 0; c < 4; ++c) a[c] = (f32x4){0.f, 0.f, 0.f, 0.f};
#pragma unroll
    for (int qn = 0; qn < 2; ++qn)
#pragma unroll
      for (int ks = 0; ks < 4; ++ks)
#pragma unroll
        for (int c = 0; c < 4; ++c) {
          a[c] = mfma16(wf[qn][ks][c], of[qn][ks], a[c]);
          wf[qn][ks][c] = wofrag(qn + 2, ks, c);
        }
#pragma unroll
    for (int c = 0; c < 4; ++c) PIN(a[c]);
#pragma unroll
    for (int i = 0; i < 32; ++i) { __builtin_amdgcn_sched_group_barrier(0x8, 1, 0); __builtin_amdgcn_sched_group_barrier(0x100, 2, 0); }
    SB();
#pragma unroll
    for (int qn = 0; qn < 2; ++qn)
#pragma unroll
      for (int ks = 0; ks < 4; ++ks)
#pragma unroll
        for (int c = 0; c < 4; ++c) a[c] = mfma16(wf[qn][ks][c], of[qn + 2][ks], a[c]);
    unsigned hb[8];
#pragma unroll
    for (int c = 0; c < 4; ++c)
#pragma unroll
      for (int m = 0; m < 2; ++m) {
        const unsigned xr = xres[c][m];
        const unsigned ab = pk2(a[c][2 * m], a[c][2 * m + 1]);
        hb[2 * c + m] = pk2(bflo(xr) + bflo(ab), bfhi(xr) + bfhi(ab));
      }
    STAMP(11);
    float s1 = 0.f, s2 = 0.f, s3 = 0.f;
#pragma unroll
    for (int m = 0; m < 8; ++m) { s1 = dot2(hb[m], ONE2, s1); s2 = dot2(hb[m], hb[m], s2); s3 = dot2(hb[m], wr[m >> 1][m & 1], s3); }
    s1 = sum32(sum16(s1)); s2 = sum32(sum16(s2)); s3 = sum32(sum16(s3));
    if (lane < 16)
      __builtin_amdgcn_raw_buffer_store_b128((v4u){asu(s1), asu(s2), asu(s3), tag ^ asu(s1) ^ asu(s2) ^ asu(s3)}, rws, sto, so_st, AUX_SC1);
    SB();
#pragma unroll
    for (int m = 0; m < 8; ++m) { hv[2 * m] = bflo(hb[m]); hv[2 * m + 1] = bfhi(hb[m]); PIN(hv[2 * m]); PIN(hv[2 * m + 1]); }
  }
  STAMP(12);
  v4u out0, out1;
  {
    float s1, s2, s3;
    for (unsigned s = 0;; ++s) {
      asm volatile("" ::: "memory");
      const v4u a = __builtin_amdgcn_raw_buffer_load_b128(rws, l16, so_st, AUX_SC1);
      const v4u c = __builtin_amdgcn_raw_buffer_load_b128(rws, l16 + 1024, so_st, AUX_SC1);
      s1 = asf(a.x) + asf(c.x); s2 = asf(a.y) + asf(c.y); s3 = asf(a.z) + asf(c.z);
      if (__all((a.x ^ a.y ^ a.z ^ a.w) == tag && (c.x ^ c.y ^ c.z ^ c.w) == tag) || s >= SPIN_CAP) break;
      __builtin_amdgcn_s_sleep(1);
    }
    STAMP(13);
    s1 = sum32(sum16(s1)); s2 = sum32(sum16(s2)); s3 = sum32(sum16(s3));
    float mb, rs;
    ln_scalars(s1, s2, mb, rs);
    const unsigned rb = pk2((s3 - mb * wrsum) * rs, 0.f) & 0xffffu;
    const unsigned ord = (rb & 0x8000u) ? (~rb & 0xffffu) : (rb | 0x8000u);
    key = (ord << 11) | keylo;
    const unsigned k1 = umax_row16(key);
    const unsigned k2 = umax_row16(key == k1 ? 0u : key);
    if (lane == 0) LDS_U2(sb + L_KEY + w * 8) = (v2u){k1, k2};
    BAR();
    unsigned hn[8];
#pragma unroll
    for (int m = 0; m < 8; ++m) {
      const unsigned d = pk2(hv[2 * m] - mb, hv[2 * m + 1] - mb);
      hn[m] = pk2(bflo(d) * rs, bfhi(d) * rs);
    }
    if (key == k1 || key == k2) {
      const unsigned base = a_cw + (key == k1 ? 0 : 256);
#pragma unroll
      for (int q = 0; q < 4; ++q) {
        const unsigned a0 = hn[2 * q], a1 = hn[2 * q + 1];
        LDS_U4(base + q * 64) = (v4u){(a0 & 0xffffu) | asu(hv[4 * q]), (a0 >> 16) | asu(hv[4 * q + 1]),
                                      (a1 & 0xffffu) | asu(hv[4 * q + 2]), (a1 >> 16) | asu(hv[4 * q + 3])};
      }
    }
    WAIT_LGKM();
    {
      const v2u e = LDS_U2(a_cr);
      __builtin_amdgcn_raw_buffer_store_b128((v4u){tag, e.x, tag, e.y}, rws, hro, so_hr, AUX_SC1);
    }
    SB();
    STAMP(14);
    unsigned ob[8];
#pragma unroll
    for (int m = 0; m < 8; ++m) ob[m] = pk2(hv[2 * m] + bflo(hn[m]), hv[2 * m + 1] + bfhi(hn[m]));
#pragma unroll
    for (int m = 0; m < 2; ++m) {
      auto r = __builtin_amdgcn_permlane16_swap(ob[m], ob[2 + m], false, false);
      auto q = __builtin_amdgcn_permlane16_swap(ob[4 + m], ob[6 + m], false, false);
      const unsigned r0 = r[0], r1 = r[1], q0 = q[0], q1 = q[1];
      out0[m] = r0; out0[2 + m] = r1; out1[m] = q0; out1[2 + m] = q1;
    }
    PIN(out0); PIN(out1);
  }
  v4u wd[11];
  if (f2 >= 0) {
    const unsigned wa2 = sb + L_FFW + (32 * w + 8 * G + q4) * 32 + p4 * 8;
#pragma unroll
    for (int i = 0; i < 11; ++i) wd[i] = tr16x2(wa2 + ((i == 10 && w == 3) ? 0 : i * 4096), 128);
    if (w == 3) wd[10] = (v4u){0u, 0u, 0u, 0u};
#pragma unroll
    for (int i = 0; i < 11; ++i) PIN(wd[i]);
  }
  RBAR();
  const v2u kk = LDS_U2(sb + L_SEL);
  const unsigned K1 = __builtin_amdgcn_readfirstlane(kk.x), K2 = __builtin_amdgcn_readfirstlane(kk.y);
  STAMP(15);
  if (key != K1 && key != K2) {
    const int col = 64 * gg + 16 * (G & 1) + 8 * (G >> 1);
    __builtin_amdgcn_raw_buffer_store_b128(out0, rout, (unsigned)(tok * 1024 + col * 2), 0, AUX_SC1);
    __builtin_amdgcn_raw_buffer_store_b128(out1, rout, (unsigned)(tok * 1024 + col * 2 + 64), 0, AUX_SC1);
  }
  if (f2 >= 0) {
    const unsigned t1 = 2047u - (K1 & 2047u), t2 = 2047u - (K2 & 2047u);
    const unsigned r1 = WS_HROWS + (t1 >> 4) * 8192, r2 = WS_HROWS + (t2 >> 4) * 8192 + ((t1 >> 4) == (t2 >> 4) ? 4096 : 0);
    v2u hg = {0u, 0u};
    float sg = 0.f;
    if (w == 0) {
      for (unsigned s = 0; s < SPIN_CAP; ++s) {
        asm volatile("" ::: "memory");
        hg = __builtin_amdgcn_raw_buffer_load_b64(rws, ((G & 1) ? r2 : r1) + (16 * f2 + r16) * 8, 0, AUX_SC1);
        if (__all(hg.x == tag)) break;
      }
      const unsigned ordk = ((G & 1) ? K2 : K1) >> 11;
      const unsigned rb = (ordk & 0x8000u) ? (ordk & 0x7fffu) : (~ordk & 0xffffu);
      sg = rbf(__builtin_amdgcn_rcpf(1.f + __builtin_amdgcn_exp2f(bflo(rb) * -1.4426950408889634f)));
      PIN(sg);
    }
    const int ksC = w + 4 * (G + 8);
    unsigned oc = (ksC < 43 ? ksC : w) * 512 + q4 * 128 + p4 * 16, oab = (w + 4 * G) * 512 + q4 * 128 + p4 * 16;
    unsigned a_b = wpriv + G * 32 + (lane & 1) * 16, a_rw = sb + L_PRIV + 8192 + (w * 2 + r16) * 64 + G * 16, a_rr = sb + L_PRIV + 8192 + (G & 1) * 64 + r16 * 4;
    unsigned oout = ((G & 1) ? t2 : t1) * 1024 + (16 * f2 + r16) * 2;
    PIN(oc); PIN(oab); PIN(a_b); PIN(a_rw); PIN(a_rr); PIN(oout);
    v4u ga, gb, gc;
    __builtin_amdgcn_s_sleep(ASLEEP);
    for (unsigned s = 0;; ++s) {
      asm volatile("" ::: "memory");
      ga = __builtin_amdgcn_raw_buffer_load_b128(rws, oab, WS_AGRAN, AUX_SC1);
      gb = __builtin_amdgcn_raw_buffer_load_b128(rws, oab, WS_AGRAN + 8192, AUX_SC1);
      gc = __builtin_amdgcn_raw_buffer_load_b128(rws, oc, WS_AGRAN, AUX_SC1);
      if (__all(ga.x == tag && ga.z == tag && gb.x == tag && gb.z == tag && gc.x == tag && gc.z == tag) || s >= SPIN_CAP) break;
      __builtin_amdgcn_s_sleep(1);
    }
    STAMP(16);
    LDS_U2(a_cr) = (v2u){ga.y, ga.w};
    LDS_U2(a_cr + 512) = (v2u){gb.y, gb.w};
    LDS_U2(a_cr + 1024) = (v2u){gc.y, gc.w};
    WAIT_LGKM();
    v4u bb[11];
#pragma unroll
    for (int i = 0; i < 11; ++i) bb[i] = LDS_U4(a_b + i * 128);
    SB();
    f32x4 d0 = {0.f, 0.f, 0.f, 0.f}, d1 = d0;
#pragma unroll
    for (int i = 0; i < 11; ++i) { if (i & 1) d1 = mfma16(wd[i], bb[i], d1); else d0 = mfma16(wd[i], bb[i], d0); }
    d0 += d1;
    if (r16 < 2) LDS_F4(a_rw) = d0;
    BAR();
    if (w == 0) {
      float y = (LDS_F1(a_rr) + LDS_F1(a_rr + 128)) + (LDS_F1(a_rr + 256) + LDS_F1(a_rr + 384));
      y = rbf(rbf(y) * sg);
      const float t = rbf(bflo(hg.y) + y);
      const unsigned ov = pk2(bfhi(hg.y) + t, 0.f) & 0xffffu;
      const unsigned nb = dppu<0xB1>(ov);
      if (lane < 32 && (lane & 1) == 0) __builtin_amdgcn_raw_buffer_store_b32(ov | (nb << 16), rout, oout, 0, AUX_SC1);
    }
    STAMP(17);
  }
  STAMP(18);
  STAMP_OUT();
  asm volatile(".Lfb_code1:");
}
}

extern "C" void kernel_launch(void* const* d_in, const int* in_sizes, int n_in, void* d_out, int out_size, void* d_ws,
                              size_t ws_size, hipStream_t stream) {
  Params p;
  p.x = (const char*)d_in[0]; p.wqkv = (const char*)d_in[1]; p.wo = (const char*)d_in[2];
  p.wr = (const char*)d_in[4]; p.wg = (const char*)d_in[5]; p.wu = (const char*)d_in[6]; p.wd = (const char*)d_in[7];
  p.out = (char*)d_out; p.ws = (char*)d_ws;
  (void)hipFuncSetAttribute((const void*)fused_block_kernel, hipFuncAttributeMaxDynamicSharedMemorySize, L_TOTAL);
  hipLaunchKernelGGL(fused_block_kernel, dim3(256), dim3(512), L_TOTAL, stream, p);
}
```

```cpp
#include <hip/hip_runtime.h>
#include <hip/hip_bf16.h>

typedef unsigned long long u64;
typedef unsigned v4u __attribute__((ext_vector_type(4)));
typedef unsigned v2u __attribute__((ext_vector_type(2)));
typedef float f32x2 __attribute__((ext_vector_type(2)));
typedef float f32x4 __attribute__((ext_vector_type(4)));
typedef float f32x16 __attribute__((ext_vector_type(16)));
typedef __bf16 bf16x2 __attribute__((ext_vector_type(2)));
typedef __bf16 bf16x4 __attribute__((ext_vector_type(4)));
typedef __bf16 bf16x8 __attribute__((ext_vector_type(8)));
#define GAS __attribute__((address_space(1)))
#define LAS __attribute__((address_space(3)))
#define DI __device__ __forceinline__
#define RLX_AGENT __ATOMIC_RELAXED, __HIP_MEMORY_SCOPE_AGENT

#define STAMPS 0

namespace {
constexpr int FF = 1376;
constexpr int AUX_SC1 = 16;
constexpr unsigned SPIN_CAP = 1u << 16;
constexpr unsigned WS_EPOCH = 0;
constexpr unsigned WS_KVFLAG = 4096;
constexpr unsigned WS_OFLAG = WS_KVFLAG + 256 * 128;
constexpr unsigned WS_KEYS = WS_OFLAG + 512 * 128;
constexpr unsigned WS_AGRAN = WS_KEYS + 256 * 128;
constexpr unsigned WS_STATS = 1u << 18;
constexpr unsigned WS_STAMP = 1u << 19;
constexpr unsigned WS_HROWS = 1u << 20;
constexpr unsigned WS_O = 2u << 20;
constexpr unsigned WS_KV = 4u << 20;
constexpr unsigned WS_BYTES = 8u << 20;
static_assert(WS_AGRAN + 192 * 128 <= WS_STATS && WS_STATS + 128 * 2048 <= WS_STAMP, "ws map");
constexpr int L_X = 0;
constexpr int L_RING = 65536;
constexpr int L_MISC = L_RING + 5 * 16384;
constexpr int L_TAB = L_MISC;
constexpr int L_XS = L_TAB + 1024;
constexpr int L_CS = L_XS + 1024;
constexpr int L_RED = L_CS + 1536;
constexpr int L_QX = L_MISC + 4096;
constexpr int L_WR = L_QX + 8192;
constexpr int L_JUNK = L_WR + 1024;
constexpr int L_SEL = L_RED + 512 - 16;
constexpr int L_FLG = L_RED;
constexpr int L_KEY = L_RED + 64;
constexpr int L_KVX = L_RING + 2 * 16384;
constexpr int L_FFW = 0;
constexpr int L_PRIV = 49152;
constexpr int L_TOTAL = 163840;
static_assert(L_JUNK + 1024 <= L_TOTAL, "lds map");

#define THR1 12
#define THR2 12
#define OSLEEP 2
#define ASLEEP 24
#define GSLEEP 8
#define FB_LIMIT 1048576.f
#define TOUCH 1
struct Params {
  const char* x; const char* wqkv; const char* wo; const char* wr; const char* wg; const char* wu; const char* wd;
  char* out; char* ws;
};

DI float asf(unsigned u) { return __builtin_bit_cast(float, u); }
DI unsigned asu(float f) { return __builtin_bit_cast(unsigned, f); }
DI float bflo(unsigned u) { return asf(u << 16); }
DI float bfhi(unsigned u) { return asf(u & 0xffff0000u); }
DI unsigned pk2(float a, float b) { f32x2 f = {a, b}; return __builtin_bit_cast(unsigned, __builtin_convertvector(f, bf16x2)); }
DI float rbf(float a) { return bflo(pk2(a, a)); }
DI float dot2(unsigned a, unsigned b, float c) {
  return __builtin_amdgcn_fdot2_f32_bf16(__builtin_bit_cast(bf16x2, a), __builtin_bit_cast(bf16x2, b), c, false);
}
DI f32x16 mfma32(v4u a, v4u b, f32x16 c) {
  return __builtin_amdgcn_mfma_f32_32x32x16_bf16(__builtin_bit_cast(bf16x8, a), __builtin_bit_cast(bf16x8, b), c, 0, 0, 0);
}
DI f32x4 mfma16(v4u a, v4u b, f32x4 c) {
  return __builtin_amdgcn_mfma_f32_16x16x32_bf16(__builtin_bit_cast(bf16x8, a), __builtin_bit_cast(bf16x8, b), c, 0, 0, 0);
}
DI float sum32(float x) {
  auto r = __builtin_amdgcn_permlane32_swap(asu(x), asu(x), false, false);
  const unsigned r0 = r[0], r1 = r[1];
  return asf(r0) + asf(r1);
}
DI float sum16(float x) {
  auto r = __builtin_amdgcn_permlane16_swap(asu(x), asu(x), false, false);
  const unsigned r0 = r[0], r1 = r[1];
  return asf(r0) + asf(r1);
}
DI float fmax32(float x) {
  auto r = __builtin_amdgcn_permlane32_swap(asu(x), asu(x), false, false);
  const unsigned r0 = r[0], r1 = r[1];
  return fmaxf(asf(r0), asf(r1));
}
template <int C> DI unsigned dppu(unsigned k) { return (unsigned)__builtin_amdgcn_update_dpp(0, (int)k, C, 0xF, 0xF, true); }
template <int C> DI float dppf(float k) { return asf(dppu<C>(asu(k))); }
DI unsigned umax_row16(unsigned k) {
  k = max(k, dppu<0xB1>(k)); k = max(k, dppu<0x4E>(k)); k = max(k, dppu<0x141>(k)); k = max(k, dppu<0x140>(k));
  return k;
}
DI unsigned umax_wave(unsigned k) {
  k = umax_row16(k);
  { auto r = __builtin_amdgcn_permlane16_swap(k, k, false, false); const unsigned r0 = r[0], r1 = r[1]; k = max(r0, r1); }
  { auto r = __builtin_amdgcn_permlane32_swap(k, k, false, false); const unsigned r0 = r[0], r1 = r[1]; k = max(r0, r1); }
  return k;
}
DI float fsum8(float v) { v += dppf<0xB1>(v); v += dppf<0x4E>(v); v += dppf<0x141>(v); return v; }
DI float fsum_row16(float v) { v = fsum8(v); v += dppf<0x140>(v); return v; }
DI void ln_scalars(float s1, float s2, float& mb, float& rs) {
  const float m = s1 * (1.f / 512.f);
  const float var = fmaxf(fmaf(-m, m, s2 * (1.f / 512.f)), 0.f);
  mb = rbf(m);
  rs = __builtin_amdgcn_rcpf(rbf(__builtin_amdgcn_sqrtf(rbf(rbf(var) + 1e-5f))));
}
DI void glds_chunk(unsigned v0, unsigned v1, const char* ba, const char* bb, unsigned m0v) {
  asm volatile("s_mov_b32 m0, %4\n\ts_nop 0\n\tglobal_load_lds_dwordx4 %0, %2\n\t"
               "s_add_u32 m0, m0, 0x1000\n\ts_nop 0\n\tglobal_load_lds_dwordx4 %1, %2\n\t"
               "s_add_u32 m0, m0, 0x1000\n\ts_nop 0\n\tglobal_load_lds_dwordx4 %0, %3\n\t"
               "s_add_u32 m0, m0, 0x1000\n\ts_nop 0\n\tglobal_load_lds_dwordx4 %1, %3"
               :: "v"(v0), "v"(v1), "s"(ba), "s"(bb), "s"(m0v) : "memory", "scc");
}
DI void glds2(unsigned v, const char* ba, const char* bb, unsigned m0v) {
  asm volatile("s_mov_b32 m0, %3\n\ts_nop 0\n\tglobal_load_lds_dwordx4 %0, %1\n\t"
               "s_add_u32 m0, m0, 0x1000\n\ts_nop 0\n\tglobal_load_lds_dwordx4 %0, %2"
               :: "v"(v), "s"(ba), "s"(bb), "s"(m0v) : "memory", "scc");
}
template <int N> DI void wait_vm() { asm volatile("s_waitcnt vmcnt(%0)" :: "n"(N) : "memory"); }
DI void glds1(unsigned v, const char* ba, unsigned m0v) {
  asm volatile("s_mov_b32 m0, %2\n\ts_nop 0\n\tglobal_load_lds_dwordx4 %0, %1" :: "v"(v), "s"(ba), "s"(m0v) : "memory");
}
#define WAIT_VM(n) asm volatile("s_waitcnt vmcnt(" #n ")" ::: "memory")
#define WAIT_LGKM() asm volatile("s_waitcnt lgkmcnt(0)" ::: "memory")
#define BAR() do { __builtin_amdgcn_sched_barrier(0); asm volatile("s_waitcnt lgkmcnt(0)\n\ts_barrier" ::: "memory"); __builtin_amdgcn_sched_barrier(0); } while (0)
#define PIN(x) asm volatile("" : "+v"(x))
#define SPIN_(x) asm volatile("" : "+s"(x))
#define SB() __builtin_amdgcn_sched_barrier(0)
#define LDS_U4(off) (*(LAS v4u*)(size_t)(off))
#define LDS_F4(off) (*(LAS f32x4*)(size_t)(off))
#define LDS_U2(off) (*(LAS v2u*)(size_t)(off))
#define LDS_F1(off) (*(LAS float*)(size_t)(off))
#define LDS_U1(off) (*(LAS unsigned*)(size_t)(off))
DI v2u tr16(unsigned off) { return __builtin_bit_cast(v2u, __builtin_amdgcn_ds_read_tr16_b64_v4bf16((LAS bf16x4*)(size_t)off)); }
DI v4u tr16x2(unsigned off, unsigned d) { const v2u a = tr16(off), b = tr16(off + d); return (v4u){a.x, a.y, b.x, b.y}; }
DI unsigned read_tag(const char* ws) {
  unsigned e;
  asm volatile("s_load_dword %0, %1, 0x0\n\ts_waitcnt lgkmcnt(0)" : "=s"(e) : "s"(ws) : "memory");
  return e + 1u;
}
DI void touch4(const char* a, const char* b, const char* c, const char* d) {
  unsigned t0, t1, t2, t3;
  asm volatile("s_load_dword %0, %4, 0x0\n\ts_load_dword %1, %5, 0x0\n\ts_load_dword %2, %6, 0x0\n\ts_load_dword %3, %7, 0x0\n\ts_waitcnt lgkmcnt(0)"
               : "=&s"(t0), "=&s"(t1), "=&s"(t2), "=&s"(t3) : "s"(a), "s"(b), "s"(c), "s"(d) : "memory");
}
DI v4u pack8(const float* o) { return (v4u){pk2(o[0], o[1]), pk2(o[2], o[3]), pk2(o[4], o[5]), pk2(o[6], o[7])}; }

#define STAMP(i) do {} while (0)
#define STAMP_OUT() do {} while (0)

#define STAMP2(i) do {} while (0)
constexpr unsigned ONE2 = 0x3F803F80u;
struct Frags { v4u x[4], a[4], b[4]; };
#define JUNKAT 6
constexpr int npieces(int n) { return (n < 8 ? 6 : (n < 12 ? 4 : 0)) + (n == JUNKAT ? 7 : 0); }
#define PRO 2
constexpr int last_before(int j) { const int r = PRO - 1 + 2 * j; return r < j + 3 ? r : j + 3; }
constexpr int younger(int j) { int s = 0; for (int n = j + 1; n <= last_before(j); ++n) s += npieces(n); return s; }

DI void lds_arrive(unsigned addr, int lane) {
  if (lane == 0) __hip_atomic_fetch_add((LAS unsigned*)(size_t)addr, 1u, __ATOMIC_RELAXED, __HIP_MEMORY_SCOPE_WORKGROUP);
}
DI void lds_wait4(unsigned addr) {
  for (unsigned s = 0; s < (SPIN_CAP << 4); ++s) {
    const unsigned v = __hip_atomic_load((LAS unsigned*)(size_t)addr, __ATOMIC_RELAXED, __HIP_MEMORY_SCOPE_WORKGROUP);
    if (__builtin_amdgcn_readfirstlane(v) >= 4u) break;
    __builtin_amdgcn_s_sleep(3);
  }
  asm volatile("" ::: "memory");
}
#define RBAR() asm volatile("s_barrier" ::: "memory")

__global__ __launch_bounds__(512, 2) void fused_block_kernel(Params p) {
  extern __shared__ __attribute__((aligned(16))) char smem[];
  const int lane = threadIdx.x & 63;
  const int wv = __builtin_amdgcn_readfirstlane(threadIdx.x >> 6);
  const int w = wv & 3, tid = threadIdx.x & 255;
  const int b = blockIdx.x, xcd = b & 7, slot = b >> 3;
  const int tt = (xcd & 3) * 8 + (slot & 7), gg = (xcd >> 2) * 4 + (slot >> 3);
  const unsigned sb = (unsigned)(size_t)(LAS char*)smem;
  const int rot = (tt + 2 * gg) & 7;
  const int f1 = slot < 22 ? xcd * 22 + slot : 999;
  const int f2 = (slot >= 22 && slot < 26) ? xcd * 4 + slot - 22 : -1;
  const int r32 = lane & 31, h = lane >> 5, r16 = lane & 15, G = lane >> 4, q4 = r16 >> 2, p4 = lane & 3;
  const int wrow = tid >> 3;
  const unsigned myblk = (unsigned)(tt * 8 + gg);
  const unsigned ov0 = (unsigned)(wrow * 1024 + gg * 128 + (((tid & 7) ^ (((wrow >> 1) & 3) << 1)) << 4)), ov1 = ov0 + 32 * 1024;
  auto issue_wo = [&](int qn) {
    const char* s0 = p.wo + (size_t)(qn * 128) * 1024;
    glds_chunk(ov0, ov1, s0, s0 + 64 * 1024, sb + L_RING + w * 1024 + ((qn + 3) % 5) * 16384);
  };

  if (__builtin_expect(wv < 4, 1)) {
    {
      const unsigned xv = (unsigned)(wrow * 1024 + (((tid & 7) ^ ((wrow >> 1) & 7)) << 4));
      const unsigned wv0 = (unsigned)(wrow * 3072 + gg * 128 + (((tid & 7) ^ (((wrow >> 1) & 1) << 2)) << 4)), wv1 = wv0 + 32 * 3072;
      const unsigned ringw = sb + L_RING + w * 1024;
      const char* xb = p.x + (size_t)tt * 65536;
      auto issue = [&](int n) {
        if (n == JUNKAT) {
          const int u = b * 4 + w, u2 = 1024 + (u % 352);
          glds1(ov0, p.wo + (size_t)(tt & 7) * 65536, sb + L_JUNK);
          glds1(ov1, p.wo + (size_t)(tt & 7) * 65536, sb + L_JUNK);
          glds1((unsigned)lane * 16, p.wg + (size_t)u * 1024, sb + L_JUNK);
          glds1((unsigned)lane * 16, p.wu + (size_t)u * 1024, sb + L_JUNK);
          glds1((unsigned)lane * 16, p.wd + (size_t)u * 1024, sb + L_JUNK);
          glds1((unsigned)lane * 16, (u < 352 ? p.wg : u < 704 ? p.wu : p.wd) + (size_t)u2 * 1024, sb + L_JUNK);
          glds1((unsigned)lane * 16, p.wd + (size_t)(1024 + 320 + (u & 31)) * 1024, sb + L_JUNK);
        }
        if (n < 8) {
          const int c = (n + rot) & 7;
          glds2(xv, xb + c * 128, xb + c * 128 + 32768, sb + L_X + n * 8192 + w * 1024);
          const char* s0 = p.wqkv + (size_t)(c * 64) * 3072 + 1024;
          glds_chunk(wv0, wv1, s0, s0 + 1024, ringw + (n % 5) * 16384);
        } else if (n < 12) {
          glds_chunk(wv0, wv1, p.wqkv + (size_t)(((2 * n + rot) & 7) * 64) * 3072, p.wqkv + (size_t)(((2 * n + 1 + rot) & 7) * 64) * 3072,
                     ringw + (n % 5) * 16384);
        }
      };
      asm volatile(".Lfb_codeA:");
      STAMP(0);
#pragma unroll
      for (int n = 0; n < PRO; ++n) issue(n);
      STAMP(19);
#define LSTEP(j) do { STAMP2(48 + (j)); wait_vm<younger(j)>(); STAMP2(32 + (j)); RBAR(); \
        _Pragma("unroll") for (int n = last_before(j) + 1; n <= last_before((j) + 1); ++n) issue(n); } while (0)
      LSTEP(0); LSTEP(1); LSTEP(2); LSTEP(3); LSTEP(4); LSTEP(5); LSTEP(6); LSTEP(7);
    }
    const unsigned tag = read_tag(p.ws);
    const __amdgpu_buffer_rsrc_t rws = __builtin_amdgcn_make_buffer_rsrc(p.ws, 0, WS_BYTES, 0x00020000);
    RBAR();
    unsigned so_ow = WS_O + (tt * 64 + gg * 2 + (w >> 1)) * 1024, so_ofw = WS_OFLAG + (tt * 16 + gg) * 128, so_kvf = WS_KVFLAG + myblk * 128;
    unsigned sot[4];
#pragma unroll
    for (int t = 0; t < 4; ++t) { sot[t] = WS_KV + (tt - 4 + t >= 0 ? myblk - (4 - t) * 8 : myblk) * 16384; SPIN_(sot[t]); }
    SPIN_(so_ow); SPIN_(so_ofw); SPIN_(so_kvf);
    unsigned kvfo;
    {
      const int st = tt - 4 + (lane & 3);
      kvfo = WS_KVFLAG + ((st < 0 ? tt : st) * 8 + gg) * 128;
    }
    unsigned lo = w * 1024 + lane * 16, lo1 = lo + 4096, lo2 = lo + 8192, lo3 = lo + 12288;
    unsigned a_q = sb + L_QX + lo, a_kv = sb + L_KVX + lo;
    unsigned ooff0 = ((unsigned)(r32 >> 4) * 16) * 1024 + (r16 + 16 * ((w & 1) * 2 + h)) * 16, ooff1 = ooff0 + 32768;
    PIN(kvfo); PIN(lo); PIN(lo1); PIN(lo2); PIN(lo3); PIN(a_q); PIN(a_kv); PIN(ooff0); PIN(ooff1);
    wait_vm<8>(); RBAR();
    unsigned pm[8];
#pragma unroll
    for (int i = 0; i < 8; ++i) {
      const int k0 = ((2 * i) & 3) + 8 * (i >> 1) + 4 * h;
      pm[i] = (k0 <= r32 ? 0xffffu : 0u) | (k0 + 1 <= r32 ? 0xffff0000u : 0u);
      PIN(pm[i]);
    }
    u64 vis[16];
#pragma unroll
    for (int i = 0; i < 16; ++i) {
      const int c0 = (i & 3) + 8 * (i >> 2);
      vis[i] = (u64)(0xffffffffu << c0) | ((u64)(0xffffffffu << (c0 + 4)) << 32);
      SPIN_(vis[i]);
    }
    auto sel = [&](int i, float yes, float no) {
      float r;
      asm("v_cndmask_b32_e64 %0, %1, %2, %3" : "=v"(r) : "v"(no), "v"(yes), "s"(vis[i]));
      return r;
    };
    const int first = 8 - 2 * tt;
    f32x16 cz;
    auto fill = [&](f32x16& blk, float v) {
      f32x2 v2 = {v, v};
      PIN(v2);
#pragma unroll
      for (int i = 0; i < 16; i += 2) { blk[i] = v2[0]; blk[i + 1] = v2[1]; }
      PIN(blk);
    };
    fill(cz, first > 0 ? -1e30f : 0.f);
    v4u kf[10], va[10], vb[10], qf[2];
#pragma unroll
    for (int k = 0; k < 10; ++k) { vb[k] = (v4u){ONE2, ONE2, ONE2, ONE2}; PIN(vb[k]); }
    WAIT_VM(0); RBAR();
    if (threadIdx.x == 0) __builtin_amdgcn_raw_buffer_store_b32(tag, rws, 0, so_kvf, AUX_SC1);
    issue_wo(0); issue_wo(1);
    STAMP(3);
    RBAR();
    unsigned fl = __builtin_amdgcn_raw_buffer_load_b32(rws, kvfo, 0, AUX_SC1);
    kf[8] = LDS_U4(a_kv); kf[9] = LDS_U4(a_kv + 8192);
    va[8] = LDS_U4(a_kv + 4096); va[9] = LDS_U4(a_kv + 12288);
    auto vfix = [&](int k) {
#pragma unroll
      for (int j = 0; j < 4; ++j) {
        auto r = __builtin_amdgcn_permlane16_swap(va[k][j], vb[k][j], false, false);
        const unsigned r0 = r[0], r1 = r[1];
        va[k][j] = r0; vb[k][j] = r1;
      }
      PIN(va[k]); PIN(vb[k]);
    };
    vfix(8); vfix(9);
    SB();
    for (unsigned s = 0; s < SPIN_CAP; ++s) {
      if (__all(fl == tag)) break;
      __builtin_amdgcn_s_sleep(1);
      asm volatile("" ::: "memory");
      fl = __builtin_amdgcn_raw_buffer_load_b32(rws, kvfo, 0, AUX_SC1);
    }
    asm volatile("" ::: "memory");
    STAMP(4);
#pragma unroll
    for (int k = 0; k < 8; ++k) kf[k] = __builtin_amdgcn_raw_buffer_load_b128(rws, (k & 1) ? lo2 : lo, sot[k >> 1], AUX_SC1);
    SB();
    RBAR();
    qf[0] = LDS_U4(a_q); qf[1] = LDS_U4(a_q + 4096);
#pragma unroll
    for (int k = 0; k < 8; ++k) va[k] = __builtin_amdgcn_raw_buffer_load_b128(rws, (k & 1) ? lo3 : lo1, sot[k >> 1], AUX_SC1);
    SB();
    RBAR();
    auto oflag = [&](int qb) {
      WAIT_VM(0);
      if (lane == 0) {
        const unsigned n = __hip_atomic_fetch_add((LAS unsigned*)(size_t)(sb + L_FLG + 8 + qb * 4), 1u, __ATOMIC_RELAXED, __HIP_MEMORY_SCOPE_WORKGROUP);
        if (n == 3u) __builtin_amdgcn_raw_buffer_store_b32(tag, rws, qb * 1024, so_ofw, AUX_SC1);
      }
    };
    STAMP(5);
    f32x16 zero;
#pragma unroll
    for (int i = 0; i < 16; ++i) zero[i] = 0.f;
    auto mx16 = [&](float& ma, float& mb, const f32x16& s) {
#pragma unroll
      for (int i = 0; i < 16; i += 4) { ma = fmaxf(fmaxf(ma, s[i]), s[i + 1]); mb = fmaxf(fmaxf(mb, s[i + 2]), s[i + 3]); }
      PIN(ma); PIN(mb);
    };
    {
      f32x16 sm[2];
      sm[0] = mfma32(kf[8], qf[0], zero); PIN(sm[0]);
      f32x16 s0 = mfma32(kf[0], qf[0], cz); PIN(s0);
      sm[1] = mfma32(kf[9], qf[1], zero); PIN(sm[1]);
      f32x16 s1 = mfma32(kf[1], qf[1], cz); PIN(s1);
      asm volatile("s_nop 7\n\ts_nop 1");
      SB();
#pragma unroll
      for (int i = 0; i < 16; ++i) sm[0][i] = sel(i, sm[0][i], s0[i]);
#pragma unroll
      for (int i = 0; i < 16; ++i) sm[1][i] = sel(i, sm[1][i], s1[i]);
      SB();
      float m0, m1;
      {
        float a0 = -3e38f, b0 = -3e38f, a1 = -3e38f, b1 = -3e38f;
        mx16(a0, b0, sm[0]); mx16(a1, b1, sm[1]);
        auto r = __builtin_amdgcn_permlane32_swap(asu(fmaxf(a0, b0)), asu(fmaxf(a1, b1)), false, false);
        const unsigned r0 = r[0], r1 = r[1];
        const float mm = fmaxf(asf(r0), asf(r1));
        auto q = __builtin_amdgcn_permlane32_swap(asu(mm), asu(mm), false, false);
        const unsigned q0 = q[0], q1 = q[1];
        m0 = asf(q0); m1 = asf(q1);
      }
#pragma unroll
      for (int k = 0; k < 8; ++k) vfix(k);
      STAMP(6);
#pragma unroll
      for (int qb = 0; qb < 2; ++qb) {
        float mx = qb ? m1 : m0;
        f32x16 o;
#pragma unroll 1
        for (int att = 0;; ++att) {
          v4u pp;
          fill(cz, first > 1 + qb ? -1e30f : -mx);
          SB();
          s0 = mfma32(kf[1 + qb], qf[qb], cz);
          PIN(s0);
          SB();
          {
            float e[16];
#pragma unroll
            for (int i = 0; i < 16; ++i) e[i] = __builtin_amdgcn_exp2f(sm[qb][i] - mx);
            const v4u p0 = pack8(e), p1 = pack8(e + 8);
            v4u d0, d1, f0, f1_;
#pragma unroll
            for (int j = 0; j < 4; ++j) { d0[j] = p0[j] & pm[j]; f0[j] = p0[j] ^ d0[j]; d1[j] = p1[j] & pm[4 + j]; f1_[j] = p1[j] ^ d1[j]; }
            o = mfma32(va[8 + qb], d0, zero);
            o = mfma32(va[qb], f0, o);
            o = mfma32(vb[8 + qb], d1, o);
            pp = f1_;
            PIN(pp); PIN(o);
          }
          SB();
#pragma unroll
          for (int k = 1 + qb; k < 8 + qb; ++k) {
            float e[16];
            if (k + 1 < 8 + qb && (k & 1) == 1 && __builtin_expect(first == k + 1, 0)) fill(cz, -mx);
#pragma unroll
            for (int i = 0; i < 4; ++i) e[i] = __builtin_amdgcn_exp2f(s0[i]);
            SB();
            o = mfma32(vb[k - 1], pp, o);
            PIN(o);
            SB();
#pragma unroll
            for (int i = 4; i < 8; ++i) e[i] = __builtin_amdgcn_exp2f(s0[i]);
            SB();
            if (k + 1 < 8 + qb) { s1 = mfma32(kf[k + 1], qf[qb], cz); PIN(s1); }
            SB();
            v4u p0 = pack8(e);
            PIN(p0);
            SB();
            e[8] = __builtin_amdgcn_exp2f(s0[8]); e[9] = __builtin_amdgcn_exp2f(s0[9]);
            SB();
            o = mfma32(va[k], p0, o);
            PIN(o);
            SB();
#pragma unroll
            for (int i = 10; i < 16; ++i) e[i] = __builtin_amdgcn_exp2f(s0[i]);
            pp = pack8(e + 8);
            PIN(pp);
            if (qb == 1 && k == 5 && att == 0) oflag(0);
            SB();
            s0 = s1;
          }
          o = mfma32(vb[7 + qb], pp, o);
          if (__builtin_expect(__all(o[8] < FB_LIMIT) || att == 1, 1)) break;
          float ta = mx, tb_ = mx;
#pragma unroll
          for (int k = 1 + qb; k < 8 + qb; ++k)
            if (k >= first) mx16(ta, tb_, mfma32(kf[k], qf[qb], zero));
          mx = fmax32(fmaxf(ta, tb_));
        }
        const float inv = __builtin_amdgcn_rcpf(o[8]);
        float on[8];
#pragma unroll
        for (int i = 0; i < 8; ++i) on[i] = o[i] * inv;
        __builtin_amdgcn_raw_buffer_store_b128(pack8(on), rws, qb ? ooff1 : ooff0, so_ow, AUX_SC1);
      }
    }
    STAMP(7);
    oflag(1);
    STAMP(8);
    if (w != 0) { STAMP_OUT(); return; }
    v4u a[16];
    if (f1 < 172) {
      lds_wait4(sb + L_FLG);
      const unsigned wa = sb + L_FFW + (p4 >> 1) * 8192 + (8 * G + q4) * 16 + (p4 & 1) * 8;
#pragma unroll
      for (int i = 0; i < 16; ++i) a[i] = tr16x2(wa + i * 512, 64);
#pragma unroll
      for (int i = 0; i < 16; ++i) PIN(a[i]);
    }
    unsigned so_keyr = WS_KEYS + xcd * 4096, so_key = WS_KEYS + tt * 128, kro = r32 * 128, l16 = lane * 16, st_w = sb + L_PRIV + 8192 + lane * 4, st_r = sb + L_PRIV + 8192 + (lane & 1) * 1024 + G * 16;
    SPIN_(so_keyr); SPIN_(so_key); PIN(kro); PIN(l16); PIN(st_w); PIN(st_r);
    RBAR();
    if (gg == 0) {
      const unsigned kq = LDS_U1(sb + L_KEY + (lane & 7) * 4);
      unsigned k1 = max(kq, dppu<0xB1>(kq)); k1 = max(k1, dppu<0x4E>(k1)); k1 = max(k1, dppu<0x141>(k1));
      unsigned k2 = kq == k1 ? 0u : kq; k2 = max(k2, dppu<0xB1>(k2)); k2 = max(k2, dppu<0x4E>(k2)); k2 = max(k2, dppu<0x141>(k2));
      if (lane < 8) __builtin_amdgcn_raw_buffer_store_b128((v4u){tag, k1, tag, k2}, rws, lane * 4096, so_key, AUX_SC1);
    }
    unsigned K1, K2;
    {
      v4u ka;
      for (unsigned s = 0;; ++s) {
        asm volatile("" ::: "memory");
        ka = __builtin_amdgcn_raw_buffer_load_b128(rws, kro, so_keyr, AUX_SC1);
        if (__all(ka.x == tag && ka.z == tag) || s >= SPIN_CAP) break;
        __builtin_amdgcn_s_sleep(2);
      }
      const unsigned kk = h ? ka.w : ka.y;
      K1 = __builtin_amdgcn_readfirstlane(umax_wave(kk));
      K2 = __builtin_amdgcn_readfirstlane(umax_wave(kk == K1 ? 0u : kk));
    }
    STAMP(15);
    const unsigned g1 = (2047u - (K1 & 2047u)) >> 4, g2 = (2047u - (K2 & 2047u)) >> 4;
    const unsigned r1 = WS_HROWS + g1 * 8192, r2 = WS_HROWS + g2 * 8192 + (g1 == g2 ? 4096 : 0);
    v4u g[8];
    if (f1 < 172) {
#pragma unroll
      for (int i = 0; i < 8; ++i) g[i] = __builtin_amdgcn_raw_buffer_load_b128(rws, l16 + (i & 3) * 1024, i < 4 ? r1 : r2, AUX_SC1);
    }
    if (lane == 0) LDS_U2(sb + L_SEL) = (v2u){K1, K2};
    asm volatile("s_waitcnt lgkmcnt(0)\n\ts_barrier" ::: "memory");
    if (b == 255 && lane == 0) __builtin_amdgcn_raw_buffer_store_b32(tag, rws, 0, WS_EPOCH, AUX_SC1);
    if (f1 < 172) {
      for (unsigned s = 0;; ++s) {
        bool ok = true;
#pragma unroll
        for (int i = 0; i < 8; ++i) ok = ok && g[i].x == tag && g[i].z == tag;
        if (__all(ok) || s >= SPIN_CAP) break;
        asm volatile("" ::: "memory");
#pragma unroll
        for (int i = 0; i < 8; ++i) g[i] = __builtin_amdgcn_raw_buffer_load_b128(rws, l16 + (i & 3) * 1024, i < 4 ? r1 : r2, AUX_SC1);
      }
#pragma unroll
      for (int i = 0; i < 8; ++i) LDS_U1(st_w + i * 256) = __builtin_amdgcn_perm(g[i].w, g[i].y, 0x05040100u);
      WAIT_LGKM();
      v4u bb[16];
#pragma unroll
      for (int i = 0; i < 16; ++i) bb[i] = LDS_U4(st_r + i * 64);
      SB();
      f32x4 d0 = {0.f, 0.f, 0.f, 0.f}, d1 = d0;
#pragma unroll
      for (int i = 0; i < 16; i += 2) { d0 = mfma16(a[i], bb[i], d0); d1 = mfma16(a[i + 1], bb[i + 1], d1); }
      d0 += d1;
      float av[4];
#pragma unroll
      for (int j = 0; j < 4; ++j) {
        auto r = __builtin_amdgcn_permlane32_swap(asu(d0[j]), asu(d0[j]), false, false);
        const unsigned r0 = r[0], r1_ = r[1];
        const float gs = rbf(asf(r0)), us = rbf(asf(r1_));
        av[j] = rbf(gs * __builtin_amdgcn_rcpf(1.f + __builtin_amdgcn_exp2f(gs * -1.4426950408889634f))) * us;
      }
      if (lane < 32 && r16 < 2)
        __builtin_amdgcn_raw_buffer_store_b128((v4u){tag, pk2(av[0], av[1]), tag, pk2(av[2], av[3])}, rws, r16 * 32 + G * 16, WS_AGRAN + f1 * 128, AUX_SC1);
      STAMP(16);
    }
    STAMP_OUT();
    asm volatile(".Lfb_code2:");
    return;
  }

  STAMP(0);
  {
    const char* pc;
    int lo_, hi_;
    asm volatile("s_getpc_b64 %0\n.Lfb_code0:\n\ts_mov_b32 %1, .Lfb_codeA-.Lfb_code0\n\ts_mov_b32 %2, .Lfb_code1-.Lfb_code0\n\t"
                 "s_max_i32 %2, %2, .Lfb_code2-.Lfb_code0\n\ts_min_i32 %1, %1, 0" : "=s"(pc), "=s"(lo_), "=s"(hi_) :: "scc");
    const int off = min((slot * 4 + w) * 1024, hi_ - lo_ - 1024);
    if ((slot * 4 + w) * 1024 < hi_ - lo_) glds1((unsigned)lane * 16, pc + lo_ + off, sb + L_JUNK);
  }
  if (w == 0) touch4(p.ws + WS_KV + myblk * 16384, p.ws + WS_KV + (tt >= 4 ? myblk - 32 : myblk) * 16384, p.ws + WS_O + tt * 65536, p.out + (size_t)tt * 65536);
  if (w == 1) touch4(p.ws + WS_STATS + tt * 8192, p.ws + WS_HROWS + tt * 32768, p.ws + WS_KEYS, p.wo + (size_t)slot * 16384);
  if (w == 2) touch4(p.wg + (size_t)slot * 44032, p.wu + (size_t)slot * 44032, p.wd + (size_t)slot * 44032, p.ws + WS_KV + (tt >= 2 ? myblk - 16 : myblk) * 16384);
  if (w == 3) glds1((unsigned)lane * 16, p.wr, sb + L_WR);
  if (tid < 4) LDS_U1(sb + L_FLG + tid * 4) = 0u;
  const unsigned tag = read_tag(p.ws);
  const __amdgpu_buffer_rsrc_t rws = __builtin_amdgcn_make_buffer_rsrc(p.ws, 0, WS_BYTES, 0x00020000);
  const int tb = w & 1, ct = w >> 1;
  unsigned so_kv = WS_KV + myblk * 16384;
  SPIN_(so_kv);
  unsigned xa[4], wa[4], wb[4];
#pragma unroll
  for (int i = 0; i < 4; ++i) {
    const int ks = i ^ (ct | (tb << 1));
    xa[i] = sb + L_X + (tb * 32 + r32) * 128 + (((2 * ks + h) ^ ((r32 >> 1) & 7)) << 4);
    wa[i] = sb + L_RING + (16 * ks + 8 * h + q4) * 128 + ((ct ^ ((q4 >> 1) & 1)) << 6) + (G & 1) * 32 + p4 * 8;
    wb[i] = wa[i] + 49152;
    PIN(xa[i]); PIN(wa[i]); PIN(wb[i]);
  }
  unsigned a_csw = sb + L_CS + h * 512 + w * 128 + r32 * 4, a_xsw = sb + L_XS + w * 256 + r32 * 8 + h * 4;
  unsigned a_xsr = sb + L_XS + tb * 256 + r32 * 8, a_csr = sb + L_CS + ct * 256 + h * 16, a_cvr = sb + L_CS + 512 + ct * 256 + r32 * 4;
  unsigned a_tabw = sb + L_TAB + w * 256 + lane * 4, a_tabr = sb + L_TAB + w * 256 + h * 16, a_cqw = sb + L_CS + 1024 + w * 128 + r32 * 4;
  unsigned ko = tb * 8192 + 2 * ct * 1024 + lane * 16;
  unsigned vo = tb * 8192 + 4096 + (2 * ct + (r32 >> 4)) * 1024 + (h * 32 + r16) * 16;
  unsigned a_kx = sb + L_KVX + ko, a_vx = sb + L_KVX + vo, a_qw = sb + L_QX + (tb * 4 + 2 * ct) * 1024 + lane * 16;
  unsigned a_xres = sb + L_X + ((gg - rot) & 7) * 8192 + (w * 16 + r16) * 128 + (((G >> 1) ^ ((r16 >> 1) & 7)) << 4) + (G & 1) * 8;
  PIN(a_csw); PIN(a_xsw); PIN(a_xsr); PIN(a_csr); PIN(a_cvr); PIN(a_tabw); PIN(a_tabr); PIN(a_cqw);
  PIN(ko); PIN(vo); PIN(a_kx); PIN(a_vx); PIN(a_qw); PIN(a_xres);
  f32x16 acc0, acc1;
#pragma unroll
  for (int i = 0; i < 16; ++i) { acc0[i] = 0.f; acc1[i] = 0.f; }
  float cs0 = 0.f, cs1 = 0.f, xs = 0.f, xq = 0.f;
  auto rd = [&](Frags& f, int slab, int sl) {
#pragma unroll
    for (int i = 0; i < 4; ++i) {
      const unsigned wo = sl < 3 ? wa[i] + sl * 16384 : wb[i] + (sl - 3) * 16384;
      f.x[i] = LDS_U4(xa[i] + slab * 8192); f.a[i] = tr16x2(wo, 512); f.b[i] = tr16x2(wo + 8192, 512);
    }
  };
  auto sums = [&](const Frags& f, bool withx) {
    PIN(acc0);
    if (withx) PIN(acc1);
#pragma unroll
    for (int i = 0; i < 8; ++i) { __builtin_amdgcn_sched_group_barrier(0x8, 1, 0); __builtin_amdgcn_sched_group_barrier(0x100, 3, 0); }
    SB();
#pragma unroll
    for (int j = 0; j < 4; ++j) {
      cs0 = dot2(f.a[0][j], ONE2, cs0); cs1 = dot2(f.b[0][j], ONE2, cs1);
      if (withx) { xs = dot2(f.x[0][j], ONE2, xs); xq = dot2(f.x[0][j], f.x[0][j], xq); }
    }
#pragma unroll
    for (int j = 0; j < 4; ++j) {
      cs0 = dot2(f.a[1][j], ONE2, cs0); cs1 = dot2(f.b[1][j], ONE2, cs1);
      if (withx) { xs = dot2(f.x[2][j], ONE2, xs); xq = dot2(f.x[2][j], f.x[2][j], xq); }
    }
    PIN(cs0); PIN(cs1);
    if (withx) { PIN(xs); PIN(xq); }
    SB();
  };
  auto mmA = [&](const Frags& f) {
#pragma unroll
    for (int i = 0; i < 4; ++i) { acc0 = mfma32(f.a[i], f.x[i], acc0); acc1 = mfma32(f.x[i], f.b[i], acc1); }
  };
  Frags fa, fb2;
  PIN(acc0); PIN(acc1);
  STAMP2(32); BAR(); rd(fa, 0, 0); sums(fa, true);
  STAMP(20);
  STAMP2(33); BAR(); rd(fb2, 1, 1); mmA(fa); sums(fb2, true);
  STAMP2(34); BAR(); rd(fa, 2, 2); mmA(fb2); sums(fa, true);
  STAMP2(35); BAR(); rd(fb2, 3, 3); mmA(fa); sums(fb2, true);
  STAMP2(36); BAR(); rd(fa, 4, 4); mmA(fb2); sums(fa, true);
  STAMP2(37); BAR(); rd(fb2, 5, 0); mmA(fa); sums(fb2, true);
  STAMP2(38); BAR(); rd(fa, 6, 1); mmA(fb2); sums(fa, true);
  STAMP2(39); BAR(); rd(fb2, 7, 2); mmA(fa); sums(fb2, true);
  {
    auto rc = __builtin_amdgcn_permlane32_swap(asu(cs0), asu(cs1), false, false);
    auto rx = __builtin_amdgcn_permlane32_swap(asu(xs), asu(xq), false, false);
    const unsigned c0 = rc[0], c1 = rc[1], x0 = rx[0], x1 = rx[1];
    LDS_F1(a_csw) = asf(c0) + asf(c1);
    LDS_F1(a_xsw) = asf(x0) + asf(x1);
  }
  cs0 = 0.f; cs1 = 0.f;
  STAMP(27);
  BAR();
  float rs1, mr1;
  v4u xh[4];
  auto rdx = [&](int slab) {
#pragma unroll
    for (int i = 0; i < 4; ++i) xh[i] = LDS_U4(xa[i] + slab * 8192);
  };
  {
    const v2u t0 = LDS_U2(a_xsr), t1 = LDS_U2(a_xsr + 512);
    f32x4 ca[4], cb[4];
#pragma unroll
    for (int g = 0; g < 4; ++g) { ca[g] = LDS_F4(a_csr + g * 32); cb[g] = LDS_F4(a_csr + 128 + g * 32); }
    const float cv0 = LDS_F1(a_cvr), cv1 = LDS_F1(a_cvr + 128);
    SB();
#pragma unroll
    for (int i = 0; i < 4; ++i) acc0 = mfma32(fb2.a[i], fb2.x[i], acc0);
    PIN(acc0);
    float mb;
    ln_scalars(asf(t0.x) + asf(t1.x), asf(t0.y) + asf(t1.y), mb, rs1);
    mr1 = -mb * rs1;
    LDS_F1(a_tabw) = h ? mr1 : rs1;
    f32x4 r4[4], m4[4];
#pragma unroll
    for (int g = 0; g < 4; ++g) { r4[g] = LDS_F4(a_tabr + g * 32); m4[g] = LDS_F4(a_tabr + 128 + g * 32); }
    SB();
#pragma unroll
    for (int i = 0; i < 4; ++i) acc1 = mfma32(fb2.x[i], fb2.b[i], acc1);
    PIN(acc1);
    float o[16];
#pragma unroll
    for (int g = 0; g < 4; ++g)
#pragma unroll
      for (int j = 0; j < 4; ++j) o[4 * g + j] = fmaf(acc0[4 * g + j], rs1, mr1 * (ca[g][j] + cb[g][j]));
    const v4u k0 = pack8(o), k1 = pack8(o + 8);
#pragma unroll
    for (int i = 0; i < 4; ++i) { __builtin_amdgcn_sched_group_barrier(0x8, 1, 0); __builtin_amdgcn_sched_group_barrier(0x2, 6, 0); }
    SB();
    __builtin_amdgcn_raw_buffer_store_b128(k0, rws, ko, so_kv, AUX_SC1);
    __builtin_amdgcn_raw_buffer_store_b128(k1, rws, ko + 1024, so_kv, AUX_SC1);
    SB();
    const float cv = cv0 + cv1;
#pragma unroll
    for (int g = 0; g < 4; ++g)
#pragma unroll
      for (int j = 0; j < 4; ++j) o[4 * g + j] = fmaf(acc1[4 * g + j], r4[g][j], m4[g][j] * cv);
    const v4u v0 = pack8(o), v1 = pack8(o + 8);
    __builtin_amdgcn_raw_buffer_store_b128(v0, rws, vo, so_kv, AUX_SC1);
    __builtin_amdgcn_raw_buffer_store_b128(v1, rws, vo + 256, so_kv, AUX_SC1);
    SB();
    STAMP(1);
    RBAR();
    rd(fa, 0, 3); rdx(1);
    LDS_U4(a_kx) = k0; LDS_U4(a_kx + 1024) = k1;
    LDS_U4(a_vx) = v0; LDS_U4(a_vx + 256) = v1;
  }
#pragma unroll
  for (int i = 0; i < 16; ++i) acc0[i] = 0.f;
  auto mmB = [&](const Frags& f) {
#pragma unroll
    for (int i = 0; i < 4; ++i) acc0 = mfma32(f.b[i], xh[i], acc0);
#pragma unroll
    for (int i = 0; i < 4; ++i) acc0 = mfma32(f.a[i], f.x[i], acc0);
  };
  sums(fa, false);
  rd(fb2, 2, 4); mmB(fa); rdx(3); sums(fb2, false);
  WAIT_VM(0);
  BAR();
  SB();
  STAMP(2);
  rd(fa, 4, 0); mmB(fb2); rdx(5); sums(fa, false);
  rd(fb2, 6, 1); mmB(fa); rdx(7);
  v2u xres[4];
#pragma unroll
  for (int c = 0; c < 4; ++c) xres[c] = LDS_U2(a_xres ^ (c * 32));
  sums(fb2, false);
  cs0 = sum32(cs0 + cs1);
  LDS_F1(a_cqw) = cs0;
  mmB(fb2);
  PIN(acc0);
  BAR();
  {
    f32x4 ca[4], cb[4];
#pragma unroll
    for (int g = 0; g < 4; ++g) { ca[g] = LDS_F4(a_csr + 1024 + g * 32); cb[g] = LDS_F4(a_csr + 1024 + 128 + g * 32); }
    SB();
    float o[16];
    const float sc = 0.25f * 1.4426950408889634f;
#pragma unroll
    for (int g = 0; g < 4; ++g)
#pragma unroll
      for (int j = 0; j < 4; ++j) o[4 * g + j] = fmaf(acc0[4 * g + j], rs1, mr1 * (ca[g][j] + cb[g][j]));
    const v4u a0 = pack8(o), a1 = pack8(o + 8);
#pragma unroll
    for (int j = 0; j < 4; ++j) { o[2 * j] = bflo(a0[j]) * sc; o[2 * j + 1] = bfhi(a0[j]) * sc; o[8 + 2 * j] = bflo(a1[j]) * sc; o[9 + 2 * j] = bfhi(a1[j]) * sc; }
    LDS_U4(a_qw) = pack8(o);
    LDS_U4(a_qw + 1024) = pack8(o + 8);
  }
  BAR();
  STAMP(3);
  const int tok = tt * 64 + w * 16 + r16;
  unsigned so_ofr = WS_OFLAG + (tt * 2 + (w >> 1)) * 1024;
  unsigned so_st = WS_STATS + (tt * 4 + w) * 2048, so_hr = WS_HROWS + (tt * 4 + w) * 8192 + gg * 512;
  unsigned oq[4];
#pragma unroll
  for (int qn = 0; qn < 4; ++qn) { oq[qn] = WS_O + (tt * 4 + w) * 16384 + qn * 4096; SPIN_(oq[qn]); }
  SPIN_(so_ofr); SPIN_(so_st); SPIN_(so_hr);
  unsigned woa[4], wob[4];
#pragma unroll
  for (int c = 0; c < 4; ++c) {
    woa[c] = sb + L_RING + (16 * (G >> 1) + 4 * (G & 1) + q4) * 128 + ((c ^ (2 * (G & 1) + (q4 >> 1))) << 5) + p4 * 8;
    wob[c] = woa[c] + 49152;
    PIN(woa[c]); PIN(wob[c]);
  }
  auto wofrag = [&](int qn, int ks, int c) { return tr16x2((qn < 2 ? wob[c] + qn * 16384 : woa[c] + (qn - 2) * 16384) + ks * 4096, 1024); };
  v2u wr[4];
#pragma unroll
  for (int c = 0; c < 4; ++c) { wr[c] = LDS_U2(sb + L_WR + gg * 128 + G * 8 + c * 32); PIN(wr[c]); }
  const v4u wrall = LDS_U4(sb + L_WR + lane * 16);
  float wrsum;
  {
    float s = 0.f;
#pragma unroll
    for (int j = 0; j < 4; ++j) s = dot2(wrall[j], ONE2, s);
    wrsum = sum32(sum16(fsum_row16(s)));
    PIN(wrsum);
  }
  const __amdgpu_buffer_rsrc_t rout = __builtin_amdgcn_make_buffer_rsrc(p.out, 0, 2048 * 1024, 0x00020000);
  const unsigned wpriv = sb + L_PRIV + w * 2048;
  unsigned l16 = lane * 16, ofo = (lane & 7) * 128, sto = lane * 16 + gg * 256;
  unsigned key, a_cw = wpriv + G * 16, a_cr = wpriv + lane * 8, hro = h * 4096 + r32 * 16, keylo = 2047 - tok;
  unsigned fv = f2 >= 0 ? (unsigned)((lane >> 1) * 1024 + (lane & 1) * 16) : (unsigned)lane * (FF * 2);
  PIN(l16); PIN(ofo); PIN(sto); PIN(a_cw); PIN(a_cr); PIN(hro); PIN(keylo); PIN(fv);
  SB();
  RBAR();
  issue_wo(2); issue_wo(3);
  if (f1 < 172) {
#pragma unroll 1
    for (int i = w * 2; i < w * 2 + 2; ++i) {
      glds1(fv, p.wg + f1 * 16 + (size_t)i * 64 * FF * 2, sb + L_FFW + i * 1024);
      wait_vm<THR1>();
      glds1(fv, p.wu + f1 * 16 + (size_t)i * 64 * FF * 2, sb + L_FFW + 8192 + i * 1024);
      wait_vm<THR1>();
    }
  } else if (f2 >= 0) {
#pragma unroll 1
    for (int i = w; i < 43; i += 4) {
      glds1(fv, p.wd + f2 * 32 + (size_t)i * 32 * 1024, sb + L_FFW + i * 1024);
      wait_vm<THR2>();
    }
  }
  STAMP(21);
  v4u wf[2][4][4];
#pragma unroll
  for (int qn = 0; qn < 2; ++qn)
#pragma unroll
    for (int ks = 0; ks < 4; ++ks)
#pragma unroll
      for (int c = 0; c < 4; ++c) wf[qn][ks][c] = wofrag(qn, ks, c);
#pragma unroll
  for (int qn = 0; qn < 2; ++qn)
#pragma unroll
    for (int ks = 0; ks < 4; ++ks)
#pragma unroll
      for (int c = 0; c < 4; ++c) PIN(wf[qn][ks][c]);
  STAMP(22);
  WAIT_VM(0);
  lds_arrive(sb + L_FLG, lane);
  STAMP(5);
  lds_wait4(sb + L_FLG);
  float hv[16];
  {
    lds_wait4(sb + L_FLG + 8 + (w >> 1) * 4);
    __builtin_amdgcn_s_sleep(OSLEEP);
    for (unsigned s = 0; s < SPIN_CAP; ++s) {
      asm volatile("" ::: "memory");
      const unsigned f = __builtin_amdgcn_raw_buffer_load_b32(rws, ofo, so_ofr, AUX_SC1);
      if (__all(f == tag)) break;
      __builtin_amdgcn_s_sleep(2);
    }
    asm volatile("" ::: "memory");
    STAMP(9);
    v4u of[4][4];
#pragma unroll
    for (int qn = 0; qn < 4; ++qn)
#pragma unroll
      for (int ks = 0; ks < 4; ++ks) of[qn][ks] = __builtin_amdgcn_raw_buffer_load_b128(rws, l16 + ks * 1024, oq[qn], AUX_SC1);
    asm volatile("" ::: "memory");
    SB();
    f32x4 a[4];
#pragma unroll
    for (int c = 0; c < 4; ++c) a[c] = (f32x4){0.f, 0.f, 0.f, 0.f};
#pragma unroll
    for (int qn = 0; qn < 2; ++qn)
#pragma unroll
      for (int ks = 0; ks < 4; ++ks)
#pragma unroll
        for (int c = 0; c < 4; ++c) {
          a[c] = mfma16(wf[qn][ks][c], of[qn][ks], a[c]);
          wf[qn][ks][c] = wofrag(qn + 2, ks, c);
        }
#pragma unroll
    for (int c = 0; c < 4; ++c) PIN(a[c]);
#pragma unroll
    for (int i = 0; i < 32; ++i) { __builtin_amdgcn_sched_group_barrier(0x8, 1, 0); __builtin_amdgcn_sched_group_barrier(0x100, 2, 0); }
    SB();
#pragma unroll
    for (int qn = 0; qn < 2; ++qn)
#pragma unroll
      for (int ks = 0; ks < 4; ++ks)
#pragma unroll
        for (int c = 0; c < 4; ++c) a[c] = mfma16(wf[qn][ks][c], of[qn + 2][ks], a[c]);
    unsigned hb[8];
#pragma unroll
    for (int c = 0; c < 4; ++c)
#pragma unroll
      for (int m = 0; m < 2; ++m) {
        const unsigned xr = xres[c][m];
        const unsigned ab = pk2(a[c][2 * m], a[c][2 * m + 1]);
        hb[2 * c + m] = pk2(bflo(xr) + bflo(ab), bfhi(xr) + bfhi(ab));
      }
    STAMP(11);
    float s1 = 0.f, s2 = 0.f, s3 = 0.f;
#pragma unroll
    for (int m = 0; m < 8; ++m) { s1 = dot2(hb[m], ONE2, s1); s2 = dot2(hb[m], hb[m], s2); s3 = dot2(hb[m], wr[m >> 1][m & 1], s3); }
    s1 = sum32(sum16(s1)); s2 = sum32(sum16(s2)); s3 = sum32(sum16(s3));
    if (lane < 16)
      __builtin_amdgcn_raw_buffer_store_b128((v4u){asu(s1), asu(s2), asu(s3), tag ^ asu(s1) ^ asu(s2) ^ asu(s3)}, rws, sto, so_st, AUX_SC1);
    SB();
#pragma unroll
    for (int m = 0; m < 8; ++m) { hv[2 * m] = bflo(hb[m]); hv[2 * m + 1] = bfhi(hb[m]); PIN(hv[2 * m]); PIN(hv[2 * m + 1]); }
  }
  STAMP(12);
  v4u out0, out1;
  {
    float s1, s2, s3;
    for (unsigned s = 0;; ++s) {
      asm volatile("" ::: "memory");
      const v4u a = __builtin_amdgcn_raw_buffer_load_b128(rws, l16, so_st, AUX_SC1);
      const v4u c = __builtin_amdgcn_raw_buffer_load_b128(rws, l16 + 1024, so_st, AUX_SC1);
      s1 = asf(a.x) + asf(c.x); s2 = asf(a.y) + asf(c.y); s3 = asf(a.z) + asf(c.z);
      if (__all((a.x ^ a.y ^ a.z ^ a.w) == tag && (c.x ^ c.y ^ c.z ^ c.w) == tag) || s >= SPIN_CAP) break;
      __builtin_amdgcn_s_sleep(1);
    }
    STAMP(13);
    s1 = sum32(sum16(s1)); s2 = sum32(sum16(s2)); s3 = sum32(sum16(s3));
    float mb, rs;
    ln_scalars(s1, s2, mb, rs);
    const unsigned rb = pk2((s3 - mb * wrsum) * rs, 0.f) & 0xffffu;
    const unsigned ord = (rb & 0x8000u) ? (~rb & 0xffffu) : (rb | 0x8000u);
    key = (ord << 11) | keylo;
    const unsigned k1 = umax_row16(key);
    const unsigned k2 = umax_row16(key == k1 ? 0u : key);
    if (lane == 0) LDS_U2(sb + L_KEY + w * 8) = (v2u){k1, k2};
    BAR();
    unsigned hn[8];
#pragma unroll
    for (int m = 0; m < 8; ++m) {
      const unsigned d = pk2(hv[2 * m] - mb, hv[2 * m + 1] - mb);
      hn[m] = pk2(bflo(d) * rs, bfhi(d) * rs);
    }
    if (key == k1 || key == k2) {
      const unsigned base = a_cw + (key == k1 ? 0 : 256);
#pragma unroll
      for (int q = 0; q < 4; ++q) {
        const unsigned a0 = hn[2 * q], a1 = hn[2 * q + 1];
        LDS_U4(base + q * 64) = (v4u){(a0 & 0xffffu) | asu(hv[4 * q]), (a0 >> 16) | asu(hv[4 * q + 1]),
                                      (a1 & 0xffffu) | asu(hv[4 * q + 2]), (a1 >> 16) | asu(hv[4 * q + 3])};
      }
    }
    WAIT_LGKM();
    {
      const v2u e = LDS_U2(a_cr);
      __builtin_amdgcn_raw_buffer_store_b128((v4u){tag, e.x, tag, e.y}, rws, hro, so_hr, AUX_SC1);
    }
    SB();
    STAMP(14);
    unsigned ob[8];
#pragma unroll
    for (int m = 0; m < 8; ++m) ob[m] = pk2(hv[2 * m] + bflo(hn[m]), hv[2 * m + 1] + bfhi(hn[m]));
#pragma unroll
    for (int m = 0; m < 2; ++m) {
      auto r = __builtin_amdgcn_permlane16_swap(ob[m], ob[2 + m], false, false);
      auto q = __builtin_amdgcn_permlane16_swap(ob[4 + m], ob[6 + m], false, false);
      const unsigned r0 = r[0], r1 = r[1], q0 = q[0], q1 = q[1];
      out0[m] = r0; out0[2 + m] = r1; out1[m] = q0; out1[2 + m] = q1;
    }
    PIN(out0); PIN(out1);
  }
  v4u wd[11];
  if (f2 >= 0) {
    const unsigned wa2 = sb + L_FFW + (32 * w + 8 * G + q4) * 32 + p4 * 8;
#pragma unroll
    for (int i = 0; i < 11; ++i) wd[i] = tr16x2(wa2 + ((i == 10 && w == 3) ? 0 : i * 4096), 128);
    if (w == 3) wd[10] = (v4u){0u, 0u, 0u, 0u};
#pragma unroll
    for (int i = 0; i < 11; ++i) PIN(wd[i]);
  }
  RBAR();
  const v2u kk = LDS_U2(sb + L_SEL);
  const unsigned K1 = __builtin_amdgcn_readfirstlane(kk.x), K2 = __builtin_amdgcn_readfirstlane(kk.y);
  STAMP(15);
  if (key != K1 && key != K2) {
    const int col = 64 * gg + 16 * (G & 1) + 8 * (G >> 1);
    __builtin_amdgcn_raw_buffer_store_b128(out0, rout, (unsigned)(tok * 1024 + col * 2), 0, AUX_SC1);
    __builtin_amdgcn_raw_buffer_store_b128(out1, rout, (unsigned)(tok * 1024 + col * 2 + 64), 0, AUX_SC1);
  }
  if (f2 >= 0) {
    const unsigned t1 = 2047u - (K1 & 2047u), t2 = 2047u - (K2 & 2047u);
    const unsigned r1 = WS_HROWS + (t1 >> 4) * 8192, r2 = WS_HROWS + (t2 >> 4) * 8192 + ((t1 >> 4) == (t2 >> 4) ? 4096 : 0);
    v2u hg = {0u, 0u};
    float sg = 0.f;
    if (w == 0) {
      for (unsigned s = 0; s < SPIN_CAP; ++s) {
        asm volatile("" ::: "memory");
        hg = __builtin_amdgcn_raw_buffer_load_b64(rws, ((G & 1) ? r2 : r1) + (16 * f2 + r16) * 8, 0, AUX_SC1);
        if (__all(hg.x == tag)) break;
      }
      const unsigned ordk = ((G & 1) ? K2 : K1) >> 11;
      const unsigned rb = (ordk & 0x8000u) ? (ordk & 0x7fffu) : (~ordk & 0xffffu);
      sg = rbf(__builtin_amdgcn_rcpf(1.f + __builtin_amdgcn_exp2f(bflo(rb) * -1.4426950408889634f)));
      PIN(sg);
    }
    const int ksC = w + 4 * (G + 8);
    unsigned oc = (ksC < 43 ? ksC : w) * 512 + q4 * 128 + p4 * 16, oab = (w + 4 * G) * 512 + q4 * 128 + p4 * 16;
    unsigned a_b = wpriv + G * 32 + (lane & 1) * 16, a_rw = sb + L_PRIV + 8192 + (w * 2 + r16) * 64 + G * 16, a_rr = sb + L_PRIV + 8192 + (G & 1) * 64 + r16 * 4;
    unsigned oout = ((G & 1) ? t2 : t1) * 1024 + (16 * f2 + r16) * 2;
    PIN(oc); PIN(oab); PIN(a_b); PIN(a_rw); PIN(a_rr); PIN(oout);
    v4u ga, gb, gc;
    __builtin_amdgcn_s_sleep(ASLEEP);
    for (unsigned s = 0;; ++s) {
      asm volatile("" ::: "memory");
      ga = __builtin_amdgcn_raw_buffer_load_b128(rws, oab, WS_AGRAN, AUX_SC1);
      gb = __builtin_amdgcn_raw_buffer_load_b128(rws, oab, WS_AGRAN + 8192, AUX_SC1);
      gc = __builtin_amdgcn_raw_buffer_load_b128(rws, oc, WS_AGRAN, AUX_SC1);
      if (__all(ga.x == tag && ga.z == tag && gb.x == tag && gb.z == tag && gc.x == tag && gc.z == tag) || s >= SPIN_CAP) break;
      __builtin_amdgcn_s_sleep(1);
    }
    STAMP(16);
    LDS_U2(a_cr) = (v2u){ga.y, ga.w};
    LDS_U2(a_cr + 512) = (v2u){gb.y, gb.w};
    LDS_U2(a_cr + 1024) = (v2u){gc.y, gc.w};
    WAIT_LGKM();
    v4u bb[11];
#pragma unroll
    for (int i = 0; i < 11; ++i) bb[i] = LDS_U4(a_b + i * 128);
    SB();
    f32x4 d0 = {0.f, 0.f, 0.f, 0.f}, d1 = d0;
#pragma unroll
    for (int i = 0; i < 11; ++i) { if (i & 1) d1 = mfma16(wd[i], bb[i], d1); else d0 = mfma16(wd[i], bb[i], d0); }
    d0 += d1;
    if (r16 < 2) LDS_F4(a_rw) = d0;
    BAR();
    if (w == 0) {
      float y = (LDS_F1(a_rr) + LDS_F1(a_rr + 128)) + (LDS_F1(a_rr + 256) + LDS_F1(a_rr + 384));
      y = rbf(rbf(y) * sg);
      const float t = rbf(bflo(hg.y) + y);
      const unsigned ov = pk2(bfhi(hg.y) + t, 0.f) & 0xffffu;
      const unsigned nb = dppu<0xB1>(ov);
      if (lane < 32 && (lane & 1) == 0) __builtin_amdgcn_raw_buffer_store_b32(ov | (nb << 16), rout, oout, 0, AUX_SC1);
    }
    STAMP(17);
  }
  STAMP(18);
  STAMP_OUT();
  asm volatile(".Lfb_code1:");
}
}

extern "C" void kernel_launch(void* const* d_in, const int* in_sizes, int n_in, void* d_out, int out_size, void* d_ws,
                              size_t ws_size, hipStream_t stream) {
  Params p;
  p.x = (const char*)d_in[0]; p.wqkv = (const char*)d_in[1]; p.wo = (const char*)d_in[2];
  p.wr = (const char*)d_in[4]; p.wg = (const char*)d_in[5]; p.wu = (const char*)d_in[6]; p.wd = (const char*)d_in[7];
  p.out = (char*)d_out; p.ws = (char*)d_ws;
  (void)hipFuncSetAttribute((const void*)fused_block_kernel, hipFuncAttributeMaxDynamicSharedMemorySize, L_TOTAL);
  hipLaunchKernelGGL(fused_block_kernel, dim3(256), dim3(512), L_TOTAL, stream, p);
}
```

```cpp
#include <hip/hip_runtime.h>
#include <hip/hip_bf16.h>

typedef unsigned long long u64;
typedef unsigned v4u __attribute__((ext_vector_type(4)));
typedef unsigned v2u __attribute__((ext_vector_type(2)));
typedef float f32x2 __attribute__((ext_vector_type(2)));
typedef float f32x4 __attribute__((ext_vector_type(4)));
typedef float f32x16 __attribute__((ext_vector_type(16)));
typedef __bf16 bf16x2 __attribute__((ext_vector_type(2)));
typedef __bf16 bf16x4 __attribute__((ext_vector_type(4)));
typedef __bf16 bf16x8 __attribute__((ext_vector_type(8)));
#define GAS __attribute__((address_space(1)))
#define LAS __attribute__((address_space(3)))
#define DI __device__ __forceinline__
#define RLX_AGENT __ATOMIC_RELAXED, __HIP_MEMORY_SCOPE_AGENT

#define STAMPS 0

namespace {
constexpr int FF = 1376;
constexpr int AUX_SC1 = 16;
constexpr unsigned SPIN_CAP = 1u << 16;
constexpr unsigned WS_EPOCH = 0;
constexpr unsigned WS_KVFLAG = 4096;
constexpr unsigned WS_OFLAG = WS_KVFLAG + 256 * 128;
constexpr unsigned WS_KEYS = WS_OFLAG + 512 * 128;
constexpr unsigned WS_AGRAN = WS_KEYS + 256 * 128;
constexpr unsigned WS_STATS = 1u << 18;
constexpr unsigned WS_STAMP = 1u << 19;
constexpr unsigned WS_HROWS = 1u << 20;
constexpr unsigned WS_O = 2u << 20;
constexpr unsigned WS_KV = 4u << 20;
constexpr unsigned WS_BYTES = 8u << 20;
static_assert(WS_AGRAN + 192 * 128 <= WS_STATS && WS_STATS + 128 * 2048 <= WS_STAMP, "ws map");
constexpr int L_X = 0;
constexpr int L_RING = 65536;
constexpr int L_MISC = L_RING + 5 * 16384;
constexpr int L_TAB = L_MISC;
constexpr int L_XS = L_TAB + 1024;
constexpr int L_CS = L_XS + 1024;
constexpr int L_RED = L_CS + 1536;
constexpr int L_QX = L_MISC + 4096;
constexpr int L_WR = L_QX + 8192;
constexpr int L_JUNK = L_WR + 1024;
constexpr int L_SEL = L_RED + 512 - 16;
constexpr int L_FLG = L_RED;
constexpr int L_KEY = L_RED + 64;
constexpr int L_KVX = L_RING + 2 * 16384;
constexpr int L_FFW = 0;
constexpr int L_PRIV = 49152;
constexpr int L_TOTAL = 163840;
static_assert(L_JUNK + 1024 <= L_TOTAL, "lds map");

#define THR1 12
#define THR2 12
#define OSLEEP 2
#define ASLEEP 24
#define GSLEEP 8
#define FB_LIMIT 1048576.f
#define TOUCH 1
struct Params {
  const char* x; const char* wqkv; const char* wo; const char* wr; const char* wg; const char* wu; const char* wd;
  char* out; char* ws;
};

DI float asf(unsigned u) { return __builtin_bit_cast(float, u); }
DI unsigned asu(float f) { return __builtin_bit_cast(unsigned, f); }
DI float bflo(unsigned u) { return asf(u << 16); }
DI float bfhi(unsigned u) { return asf(u & 0xffff0000u); }
DI unsigned pk2(float a, float b) { f32x2 f = {a, b}; return __builtin_bit_cast(unsigned, __builtin_convertvector(f, bf16x2)); }
DI float rbf(float a) { return bflo(pk2(a, a)); }
DI float dot2(unsigned a, unsigned b, float c) {
  return __builtin_amdgcn_fdot2_f32_bf16(__builtin_bit_cast(bf16x2, a), __builtin_bit_cast(bf16x2, b), c, false);
}
DI f32x16 mfma32(v4u a, v4u b, f32x16 c) {
  return __builtin_amdgcn_mfma_f32_32x32x16_bf16(__builtin_bit_cast(bf16x8, a), __builtin_bit_cast(bf16x8, b), c, 0, 0, 0);
}
DI f32x4 mfma16(v4u a, v4u b, f32x4 c) {
  return __builtin_amdgcn_mfma_f32_16x16x32_bf16(__builtin_bit_cast(bf16x8, a), __builtin_bit_cast(bf16x8, b), c, 0, 0, 0);
}
DI float sum32(float x) {
  auto r = __builtin_amdgcn_permlane32_swap(asu(x), asu(x), false, false);
  const unsigned r0 = r[0], r1 = r[1];
  return asf(r0) + asf(r1);
}
DI float sum16(float x) {
  auto r = __builtin_amdgcn_permlane16_swap(asu(x), asu(x), false, false);
  const unsigned r0 = r[0], r1 = r[1];
  return asf(r0) + asf(r1);
}
DI float fmax32(float x) {
  auto r = __builtin_amdgcn_permlane32_swap(asu(x), asu(x), false, false);
  const unsigned r0 = r[0], r1 = r[1];
  return fmaxf(asf(r0), asf(r1));
}
template <int C> DI unsigned dppu(unsigned k) { return (unsigned)__builtin_amdgcn_update_dpp(0, (int)k, C, 0xF, 0xF, true); }
template <int C> DI float dppf(float k) { return asf(dppu<C>(asu(k))); }
DI unsigned umax_row16(unsigned k) {
  k = max(k, dppu<0xB1>(k)); k = max(k, dppu<0x4E>(k)); k = max(k, dppu<0x141>(k)); k = max(k, dppu<0x140>(k));
  return k;
}
DI unsigned umax_wave(unsigned k) {
  k = umax_row16(k);
  { auto r = __builtin_amdgcn_permlane16_swap(k, k, false, false); const unsigned r0 = r[0], r1 = r[1]; k = max(r0, r1); }
  { auto r = __builtin_amdgcn_permlane32_swap(k, k, false, false); const unsigned r0 = r[0], r1 = r[1]; k = max(r0, r1); }
  return k;
}
DI float fsum8(float v) { v += dppf<0xB1>(v); v += dppf<0x4E>(v); v += dppf<0x141>(v); return v; }
DI float fsum_row16(float v) { v = fsum8(v); v += dppf<0x140>(v); return v; }
DI void ln_scalars(float s1, float s2, float& mb, float& rs) {
  const float m = s1 * (1.f / 512.f);
  const float var = fmaxf(fmaf(-m, m, s2 * (1.f / 512.f)), 0.f);
  mb = rbf(m);
  rs = __builtin_amdgcn_rcpf(rbf(__builtin_amdgcn_sqrtf(rbf(rbf(var) + 1e-5f))));
}
DI void glds_chunk(unsigned v0, unsigned v1, const char* ba, const char* bb, unsigned m0v) {
  asm volatile("s_mov_b32 m0, %4\n\ts_nop 0\n\tglobal_load_lds_dwordx4 %0, %2\n\t"
               "s_add_u32 m0, m0, 0x1000\n\ts_nop 0\n\tglobal_load_lds_dwordx4 %1, %2\n\t"
               "s_add_u32 m0, m0, 0x1000\n\ts_nop 0\n\tglobal_load_lds_dwordx4 %0, %3\n\t"
               "s_add_u32 m0, m0, 0x1000\n\ts_nop 0\n\tglobal_load_lds_dwordx4 %1, %3"
               :: "v"(v0), "v"(v1), "s"(ba), "s"(bb), "s"(m0v) : "memory", "scc");
}
DI void glds2(unsigned v, const char* ba, const char* bb, unsigned m0v) {
  asm volatile("s_mov_b32 m0, %3\n\ts_nop 0\n\tglobal_load_lds_dwordx4 %0, %1\n\t"
               "s_add_u32 m0, m0, 0x1000\n\ts_nop 0\n\tglobal_load_lds_dwordx4 %0, %2"
               :: "v"(v), "s"(ba), "s"(bb), "s"(m0v) : "memory", "scc");
}
template <int N> DI void wait_vm() { asm volatile("s_waitcnt vmcnt(%0)" :: "n"(N) : "memory"); }
DI void glds1(unsigned v, const char* ba, unsigned m0v) {
  asm volatile("s_mov_b32 m0, %2\n\ts_nop 0\n\tglobal_load_lds_dwordx4 %0, %1" :: "v"(v), "s"(ba), "s"(m0v) : "memory");
}
#define WAIT_VM(n) asm volatile("s_waitcnt vmcnt(" #n ")" ::: "memory")
#define WAIT_LGKM() asm volatile("s_waitcnt lgkmcnt(0)" ::: "memory")
#define BAR() do { __builtin_amdgcn_sched_barrier(0); asm volatile("s_waitcnt lgkmcnt(0)\n\ts_barrier" ::: "memory"); __builtin_amdgcn_sched_barrier(0); } while (0)
#define PIN(x) asm volatile("" : "+v"(x))
#define SPIN_(x) asm volatile("" : "+s"(x))
#define SB() __builtin_amdgcn_sched_barrier(0)
#define LDS_U4(off) (*(LAS v4u*)(size_t)(off))
#define LDS_F4(off) (*(LAS f32x4*)(size_t)(off))
#define LDS_U2(off) (*(LAS v2u*)(size_t)(off))
#define LDS_F1(off) (*(LAS float*)(size_t)(off))
#define LDS_U1(off) (*(LAS unsigned*)(size_t)(off))
DI v2u tr16(unsigned off) { return __builtin_bit_cast(v2u, __builtin_amdgcn_ds_read_tr16_b64_v4bf16((LAS bf16x4*)(size_t)off)); }
DI v4u tr16x2(unsigned off, unsigned d) { const v2u a = tr16(off), b = tr16(off + d); return (v4u){a.x, a.y, b.x, b.y}; }
DI unsigned read_tag(const char* ws) {
  unsigned e;
  asm volatile("s_load_dword %0, %1, 0x0\n\ts_waitcnt lgkmcnt(0)" : "=s"(e) : "s"(ws) : "memory");
  return e + 1u;
}
DI void touch4(const char* a, const char* b, const char* c, const char* d) {
  unsigned t0, t1, t2, t3;
  asm volatile("s_load_dword %0, %4, 0x0\n\ts_load_dword %1, %5, 0x0\n\ts_load_dword %2, %6, 0x0\n\ts_load_dword %3, %7, 0x0\n\ts_waitcnt lgkmcnt(0)"
               : "=&s"(t0), "=&s"(t1), "=&s"(t2), "=&s"(t3) : "s"(a), "s"(b), "s"(c), "s"(d) : "memory");
}
DI v4u pack8(const float* o) { return (v4u){pk2(o[0], o[1]), pk2(o[2], o[3]), pk2(o[4], o[5]), pk2(o[6], o[7])}; }

#define STAMP(i) do {} while (0)
#define STAMP_OUT() do {} while (0)

#define STAMP2(i) do {} while (0)
constexpr unsigned ONE2 = 0x3F803F80u;
struct Frags { v4u x[4], a[4], b[4]; };
#define JUNKAT 6
constexpr int npieces(int n) { return (n < 8 ? 6 : (n < 12 ? 4 : 0)) + (n == JUNKAT ? 7 : 0); }
#define PRO 2
constexpr int last_before(int j) { const int r = PRO - 1 + 2 * j; return r < j + 3 ? r : j + 3; }
constexpr int younger(int j) { int s = 0; for (int n = j + 1; n <= last_before(j); ++n) s += npieces(n); return s; }

DI void lds_arrive(unsigned addr, int lane) {
  if (lane == 0) __hip_atomic_fetch_add((LAS unsigned*)(size_t)addr, 1u, __ATOMIC_RELAXED, __HIP_MEMORY_SCOPE_WORKGROUP);
}
DI void lds_wait4(unsigned addr) {
  for (unsigned s = 0; s < (SPIN_CAP << 4); ++s) {
    const unsigned v = __hip_atomic_load((LAS unsigned*)(size_t)addr, __ATOMIC_RELAXED, __HIP_MEMORY_SCOPE_WORKGROUP);
    if (__builtin_amdgcn_readfirstlane(v) >= 4u) break;
    __builtin_amdgcn_s_sleep(3);
  }
  asm volatile("" ::: "memory");
}
#define RBAR() asm volatile("s_barrier" ::: "memory")

__global__ __launch_bounds__(512, 2) void fused_block_kernel(Params p) {
  extern __shared__ __attribute__((aligned(16))) char smem[];
  const int lane = threadIdx.x & 63;
  const int wv = __builtin_amdgcn_readfirstlane(threadIdx.x >> 6);
  const int w = wv & 3, tid = threadIdx.x & 255;
  const int b = blockIdx.x, xcd = b & 7, slot = b >> 3;
  const int tt = (xcd & 3) * 8 + (slot & 7), gg = (xcd >> 2) * 4 + (slot >> 3);
  const unsigned sb = (unsigned)(size_t)(LAS char*)smem;
  const int rot = (tt + 2 * gg) & 7;
  const int f1 = slot < 22 ? xcd * 22 + slot : 999;
  const int f2 = (slot >= 22 && slot < 26) ? xcd * 4 + slot - 22 : -1;
  const int r32 = lane & 31, h = lane >> 5, r16 = lane & 15, G = lane >> 4, q4 = r16 >> 2, p4 = lane & 3;
  const int wrow = tid >> 3;
  const unsigned myblk = (unsigned)(tt * 8 + gg);
  const unsigned ov0 = (unsigned)(wrow * 1024 + gg * 128 + (((tid & 7) ^ (((wrow >> 1) & 3) << 1)) << 4)), ov1 = ov0 + 32 * 1024;
  auto issue_wo = [&](int qn) {
    const char* s0 = p.wo + (size_t)(qn * 128) * 1024;
    glds_chunk(ov0, ov1, s0, s0 + 64 * 1024, sb + L_RING + w * 1024 + ((qn + 3) % 5) * 16384);
  };

  if (__builtin_expect(wv < 4, 1)) {
    {
      const unsigned xv = (unsigned)(wrow * 1024 + (((tid & 7) ^ ((wrow >> 1) & 7)) << 4));
      const unsigned wv0 = (unsigned)(wrow * 3072 + gg * 128 + (((tid & 7) ^ (((wrow >> 1) & 1) << 2)) << 4)), wv1 = wv0 + 32 * 3072;
      const unsigned ringw = sb + L_RING + w * 1024;
      const char* xb = p.x + (size_t)tt * 65536;
      auto issue = [&](int n) {
        if (n == JUNKAT) {
          const int u = b * 4 + w, u2 = 1024 + (u % 352);
          glds1(ov0, p.wo + (size_t)(tt & 7) * 65536, sb + L_JUNK);
          glds1(ov1, p.wo + (size_t)(tt & 7) * 65536, sb + L_JUNK);
          glds1((unsigned)lane * 16, p.wg + (size_t)u * 1024, sb + L_JUNK);
          glds1((unsigned)lane * 16, p.wu + (size_t)u * 1024, sb + L_JUNK);
          glds1((unsigned)lane * 16, p.wd + (size_t)u * 1024, sb + L_JUNK);
          glds1((unsigned)lane * 16, (u < 352 ? p.wg : u < 704 ? p.wu : p.wd) + (size_t)u2 * 1024, sb + L_JUNK);
          glds1((unsigned)lane * 16, p.wd + (size_t)(1024 + 320 + (u & 31)) * 1024, sb + L_JUNK);
        }
        if (n < 8) {
          const int c = (n + rot) & 7;
          glds2(xv, xb + c * 128, xb + c * 128 + 32768, sb + L_X + n * 8192 + w * 1024);
          const char* s0 = p.wqkv + (size_t)(c * 64) * 3072 + 1024;
          glds_chunk(wv0, wv1, s0, s0 + 1024, ringw + (n % 5) * 16384);
        } else if (n < 12) {
          glds_chunk(wv0, wv1, p.wqkv + (size_t)(((2 * n + rot) & 7) * 64) * 3072, p.wqkv + (size_t)(((2 * n + 1 + rot) & 7) * 64) * 3072,
                     ringw + (n % 5) * 16384);
        }
      };
      asm volatile(".Lfb_codeA:");
      STAMP(0);
#pragma unroll
      for (int n = 0; n < PRO; ++n) issue(n);
      STAMP(19);
#define LSTEP(j) do { STAMP2(48 + (j)); wait_vm<younger(j)>(); STAMP2(32 + (j)); RBAR(); \
        _Pragma("unroll") for (int n = last_before(j) + 1; n <= last_before((j) + 1); ++n) issue(n); } while (0)
      LSTEP(0); LSTEP(1); LSTEP(2); LSTEP(3); LSTEP(4); LSTEP(5); LSTEP(6); LSTEP(7);
    }
    const unsigned tag = read_tag(p.ws);
    const __amdgpu_buffer_rsrc_t rws = __builtin_amdgcn_make_buffer_rsrc(p.ws, 0, WS_BYTES, 0x00020000);
    RBAR();
    unsigned so_ow = WS_O + (tt * 64 + gg * 2 + (w >> 1)) * 1024, so_ofw = WS_OFLAG + (tt * 16 + gg) * 128, so_kvf = WS_KVFLAG + myblk * 128;
    unsigned sot[4];
#pragma unroll
    for (int t = 0; t < 4; ++t) { sot[t] = WS_KV + (tt - 4 + t >= 0 ? myblk - (4 - t) * 8 : myblk) * 16384; SPIN_(sot[t]); }
    SPIN_(so_ow); SPIN_(so_ofw); SPIN_(so_kvf);
    unsigned kvfo;
    {
      const int st = tt - 4 + (lane & 3);
      kvfo = WS_KVFLAG + ((st < 0 ? tt : st) * 8 + gg) * 128;
    }
    unsigned lo = w * 1024 + lane * 16, lo1 = lo + 4096, lo2 = lo + 8192, lo3 = lo + 12288;
    unsigned a_q = sb + L_QX + lo, a_kv = sb + L_KVX + lo;
    unsigned ooff0 = ((unsigned)(r32 >> 4) * 16) * 1024 + (r16 + 16 * ((w & 1) * 2 + h)) * 16, ooff1 = ooff0 + 32768;
    PIN(kvfo); PIN(lo); PIN(lo1); PIN(lo2); PIN(lo3); PIN(a_q); PIN(a_kv); PIN(ooff0); PIN(ooff1);
    wait_vm<8>(); RBAR();
    unsigned pm[8];
#pragma unroll
    for (int i = 0; i < 8; ++i) {
      const int k0 = ((2 * i) & 3) + 8 * (i >> 1) + 4 * h;
      pm[i] = (k0 <= r32 ? 0xffffu : 0u) | (k0 + 1 <= r32 ? 0xffff0000u : 0u);
      PIN(pm[i]);
    }
    u64 vis[16];
#pragma unroll
    for (int i = 0; i < 16; ++i) {
      const int c0 = (i & 3) + 8 * (i >> 2);
      vis[i] = (u64)(0xffffffffu << c0) | ((u64)(0xffffffffu << (c0 + 4)) << 32);
      SPIN_(vis[i]);
    }
    auto sel = [&](int i, float yes, float no) {
      float r;
      asm("v_cndmask_b32_e64 %0, %1, %2, %3" : "=v"(r) : "v"(no), "v"(yes), "s"(vis[i]));
      return r;
    };
    const int first = 8 - 2 * tt;
    f32x16 cz;
    auto fill = [&](f32x16& blk, float v) {
      f32x2 v2 = {v, v};
      PIN(v2);
#pragma unroll
      for (int i = 0; i < 16; i += 2) { blk[i] = v2[0]; blk[i + 1] = v2[1]; }
      PIN(blk);
    };
    fill(cz, first > 0 ? -1e30f : 0.f);
    v4u kf[10], va[10], vb[10], qf[2];
#pragma unroll
    for (int k = 0; k < 10; ++k) { vb[k] = (v4u){ONE2, ONE2, ONE2, ONE2}; PIN(vb[k]); }
    WAIT_VM(0); RBAR();
    if (threadIdx.x == 0) __builtin_amdgcn_raw_buffer_store_b32(tag, rws, 0, so_kvf, AUX_SC1);
    issue_wo(0); issue_wo(1);
    STAMP(3);
    RBAR();
    unsigned fl = __builtin_amdgcn_raw_buffer_load_b32(rws, kvfo, 0, AUX_SC1);
    kf[8] = LDS_U4(a_kv); kf[9] = LDS_U4(a_kv + 8192);
    va[8] = LDS_U4(a_kv + 4096); va[9] = LDS_U4(a_kv + 12288);
    auto vfix = [&](int k) {
#pragma unroll
      for (int j = 0; j < 4; ++j) {
        auto r = __builtin_amdgcn_permlane16_swap(va[k][j], vb[k][j], false, false);
        const unsigned r0 = r[0], r1 = r[1];
        va[k][j] = r0; vb[k][j] = r1;
      }
      PIN(va[k]); PIN(vb[k]);
    };
    vfix(8); vfix(9);
    SB();
    for (unsigned s = 0; s < SPIN_CAP; ++s) {
      if (__all(fl == tag)) break;
      __builtin_amdgcn_s_sleep(1);
      asm volatile("" ::: "memory");
      fl = __builtin_amdgcn_raw_buffer_load_b32(rws, kvfo, 0, AUX_SC1);
    }
    asm volatile("" ::: "memory");
    STAMP(4);
#pragma unroll
    for (int k = 0; k < 8; ++k) kf[k] = __builtin_amdgcn_raw_buffer_load_b128(rws, (k & 1) ? lo2 : lo, sot[k >> 1], AUX_SC1);
    SB();
    RBAR();
    qf[0] = LDS_U4(a_q); qf[1] = LDS_U4(a_q + 4096);
#pragma unroll
    for (int k = 0; k < 8; ++k) va[k] = __builtin_amdgcn_raw_buffer_load_b128(rws, (k & 1) ? lo3 : lo1, sot[k >> 1], AUX_SC1);
    SB();
    RBAR();
    auto oflag = [&](int qb) {
      WAIT_VM(0);
      if (lane == 0) {
        const unsigned n = __hip_atomic_fetch_add((LAS unsigned*)(size_t)(sb + L_FLG + 8 + qb * 4), 1u, __ATOMIC_RELAXED, __HIP_MEMORY_SCOPE_WORKGROUP);
        if (n == 3u) __builtin_amdgcn_raw_buffer_store_b32(tag, rws, qb * 1024, so_ofw, AUX_SC1);
      }
    };
    STAMP(5);
    f32x16 zero;
#pragma unroll
    for (int i = 0; i < 16; ++i) zero[i] = 0.f;
    auto mx16 = [&](float& ma, float& mb, const f32x16& s) {
#pragma unroll
      for (int i = 0; i < 16; i += 4) { ma = fmaxf(fmaxf(ma, s[i]), s[i + 1]); mb = fmaxf(fmaxf(mb, s[i + 2]), s[i + 3]); }
      PIN(ma); PIN(mb);
    };
    {
      f32x16 sm[2];
      sm[0] = mfma32(kf[8], qf[0], zero); PIN(sm[0]);
      f32x16 s0 = mfma32(kf[0], qf[0], cz); PIN(s0);
      sm[1] = mfma32(kf[9], qf[1], zero); PIN(sm[1]);
      f32x16 s1 = mfma32(kf[1], qf[1], cz); PIN(s1);
      asm volatile("s_nop 7\n\ts_nop 1");
      SB();
#pragma unroll
      for (int i = 0; i < 16; ++i) sm[0][i] = sel(i, sm[0][i], s0[i]);
#pragma unroll
      for (int i = 0; i < 16; ++i) sm[1][i] = sel(i, sm[1][i], s1[i]);
      SB();
      float m0, m1;
      {
        float a0 = -3e38f, b0 = -3e38f, a1 = -3e38f, b1 = -3e38f;
        mx16(a0, b0, sm[0]); mx16(a1, b1, sm[1]);
        auto r = __builtin_amdgcn_permlane32_swap(asu(fmaxf(a0, b0)), asu(fmaxf(a1, b1)), false, false);
        const unsigned r0 = r[0], r1 = r[1];
        const float mm = fmaxf(asf(r0), asf(r1));
        auto q = __builtin_amdgcn_permlane32_swap(asu(mm), asu(mm), false, false);
        const unsigned q0 = q[0], q1 = q[1];
        m0 = asf(q0); m1 = asf(q1);
      }
#pragma unroll
      for (int k = 0; k < 8; ++k) vfix(k);
      STAMP(6);
#pragma unroll
      for (int qb = 0; qb < 2; ++qb) {
        float mx = qb ? m1 : m0;
        f32x16 o;
#pragma unroll 1
        for (int att = 0;; ++att) {
          v4u pp;
          fill(cz, first > 1 + qb ? -1e30f : -mx);
          SB();
          s0 = mfma32(kf[1 + qb], qf[qb], cz);
          PIN(s0);
          SB();
          {
            float e[16];
#pragma unroll
            for (int i = 0; i < 16; ++i) e[i] = __builtin_amdgcn_exp2f(sm[qb][i] - mx);
            const v4u p0 = pack8(e), p1 = pack8(e + 8);
            v4u d0, d1, f0, f1_;
#pragma unroll
            for (int j = 0; j < 4; ++j) { d0[j] = p0[j] & pm[j]; f0[j] = p0[j] ^ d0[j]; d1[j] = p1[j] & pm[4 + j]; f1_[j] = p1[j] ^ d1[j]; }
            o = mfma32(va[8 + qb], d0, zero);
            o = mfma32(va[qb], f0, o);
            o = mfma32(vb[8 + qb], d1, o);
            pp = f1_;
            PIN(pp); PIN(o);
          }
          SB();
#pragma unroll
          for (int k = 1 + qb; k < 8 + qb; ++k) {
            float e[16];
            if (k + 1 < 8 + qb && (k & 1) == 1 && __builtin_expect(first == k + 1, 0)) fill(cz, -mx);
#pragma unroll
            for (int i = 0; i < 4; ++i) e[i] = __builtin_amdgcn_exp2f(s0[i]);
            SB();
            o = mfma32(vb[k - 1], pp, o);
            PIN(o);
            SB();
#pragma unroll
            for (int i = 4; i < 8; ++i) e[i] = __builtin_amdgcn_exp2f(s0[i]);
            SB();
            if (k + 1 < 8 + qb) { s1 = mfma32(kf[k + 1], qf[qb], cz); PIN(s1); }
            SB();
            v4u p0 = pack8(e);
            PIN(p0);
            SB();
            e[8] = __builtin_amdgcn_exp2f(s0[8]); e[9] = __builtin_amdgcn_exp2f(s0[9]);
            SB();
            o = mfma32(va[k], p0, o);
            PIN(o);
            SB();
#pragma unroll
            for (int i = 10; i < 16; ++i) e[i] = __builtin_amdgcn_exp2f(s0[i]);
            pp = pack8(e + 8);
            PIN(pp);
            if (qb == 1 && k == 5 && att == 0) oflag(0);
            SB();
            s0 = s1;
          }
          o = mfma32(vb[7 + qb], pp, o);
          if (__builtin_expect(__all(o[8] < FB_LIMIT) || att == 1, 1)) break;
          float ta = mx, tb_ = mx;
#pragma unroll
          for (int k = 1 + qb; k < 8 + qb; ++k)
            if (k >= first) mx16(ta, tb_, mfma32(kf[k], qf[qb], zero));
          mx = fmax32(fmaxf(ta, tb_));
        }
        const float inv = __builtin_amdgcn_rcpf(o[8]);
        float on[8];
#pragma unroll
        for (int i = 0; i < 8; ++i) on[i] = o[i] * inv;
        __builtin_amdgcn_raw_buffer_store_b128(pack8(on), rws, qb ? ooff1 : ooff0, so_ow, AUX_SC1);
      }
    }
    STAMP(7);
    oflag(1);
    STAMP(8);
    if (w != 0) { STAMP_OUT(); return; }
    v4u a[16];
    if (f1 < 172) {
      lds_wait4(sb + L_FLG);
      const unsigned wa = sb + L_FFW + (p4 >> 1) * 8192 + (8 * G + q4) * 16 + (p4 & 1) * 8;
#pragma unroll
      for (int i = 0; i < 16; ++i) a[i] = tr16x2(wa + i * 512, 64);
#pragma unroll
      for (int i = 0; i < 16; ++i) PIN(a[i]);
    }
    unsigned so_keyr = WS_KEYS + xcd * 4096, so_key = WS_KEYS + tt * 128, kro = r32 * 128, l16 = lane * 16, st_w = sb + L_PRIV + 8192 + lane * 4, st_r = sb + L_PRIV + 8192 + (lane & 1) * 1024 + G * 16;
    SPIN_(so_keyr); SPIN_(so_key); PIN(kro); PIN(l16); PIN(st_w); PIN(st_r);
    RBAR();
    if (gg == 0) {
      const unsigned kq = LDS_U1(sb + L_KEY + (lane & 7) * 4);
      unsigned k1 = max(kq, dppu<0xB1>(kq)); k1 = max(k1, dppu<0x4E>(k1)); k1 = max(k1, dppu<0x141>(k1));
      unsigned k2 = kq == k1 ? 0u : kq; k2 = max(k2, dppu<0xB1>(k2)); k2 = max(k2, dppu<0x4E>(k2)); k2 = max(k2, dppu<0x141>(k2));
      if (lane < 8) __builtin_amdgcn_raw_buffer_store_b128((v4u){tag, k1, tag, k2}, rws, lane * 4096, so_key, AUX_SC1);
    }
    unsigned K1, K2;
    {
      v4u ka;
      for (unsigned s = 0;; ++s) {
        asm volatile("" ::: "memory");
        ka = __builtin_amdgcn_raw_buffer_load_b128(rws, kro, so_keyr, AUX_SC1);
        if (__all(ka.x == tag && ka.z == tag) || s >= SPIN_CAP) break;
        __builtin_amdgcn_s_sleep(2);
      }
      const unsigned kk = h ? ka.w : ka.y;
      K1 = __builtin_amdgcn_readfirstlane(umax_wave(kk));
      K2 = __builtin_amdgcn_readfirstlane(umax_wave(kk == K1 ? 0u : kk));
    }
    STAMP(15);
    const unsigned g1 = (2047u - (K1 & 2047u)) >> 4, g2 = (2047u - (K2 & 2047u)) >> 4;
    const unsigned r1 = WS_HROWS + g1 * 8192, r2 = WS_HROWS + g2 * 8192 + (g1 == g2 ? 4096 : 0);
    v4u g[8];
    if (f1 < 172) {
#pragma unroll
      for (int i = 0; i < 8; ++i) g[i] = __builtin_amdgcn_raw_buffer_load_b128(rws, l16 + (i & 3) * 1024, i < 4 ? r1 : r2, AUX_SC1);
    }
    if (lane == 0) LDS_U2(sb + L_SEL) = (v2u){K1, K2};
    asm volatile("s_waitcnt lgkmcnt(0)\n\ts_barrier" ::: "memory");
    if (b == 255 && lane == 0) __builtin_amdgcn_raw_buffer_store_b32(tag, rws, 0, WS_EPOCH, AUX_SC1);
    if (f1 < 172) {
      for (unsigned s = 0;; ++s) {
        bool ok = true;
#pragma unroll
        for (int i = 0; i < 8; ++i) ok = ok && g[i].x == tag && g[i].z == tag;
        if (__all(ok) || s >= SPIN_CAP) break;
        asm volatile("" ::: "memory");
#pragma unroll
        for (int i = 0; i < 8; ++i) g[i] = __builtin_amdgcn_raw_buffer_load_b128(rws, l16 + (i & 3) * 1024, i < 4 ? r1 : r2, AUX_SC1);
      }
#pragma unroll
      for (int i = 0; i < 8; ++i) LDS_U1(st_w + i * 256) = __builtin_amdgcn_perm(g[i].w, g[i].y, 0x05040100u);
      WAIT_LGKM();
      v4u bb[16];
#pragma unroll
      for (int i = 0; i < 16; ++i) bb[i] = LDS_U4(st_r + i * 64);
      SB();
      f32x4 d0 = {0.f, 0.f, 0.f, 0.f}, d1 = d0;
#pragma unroll
      for (int i = 0; i < 16; i += 2) { d0 = mfma16(a[i], bb[i], d0); d1 = mfma16(a[i + 1], bb[i + 1], d1); }
      d0 += d1;
      float av[4];
#pragma unroll
      for (int j = 0; j < 4; ++j) {
        auto r = __builtin_amdgcn_permlane32_swap(asu(d0[j]), asu(d0[j]), false, false);
        const unsigned r0 = r[0], r1_ = r[1];
        const float gs = rbf(asf(r0)), us = rbf(asf(r1_));
        av[j] = rbf(gs * __builtin_amdgcn_rcpf(1.f + __builtin_amdgcn_exp2f(gs * -1.4426950408889634f))) * us;
      }
      if (lane < 32 && r16 < 2)
        __builtin_amdgcn_raw_buffer_store_b128((v4u){tag, pk2(av[0], av[1]), tag, pk2(av[2], av[3])}, rws, r16 * 32 + G * 16, WS_AGRAN + f1 * 128, AUX_SC1);
      STAMP(16);
    }
    STAMP_OUT();
    asm volatile(".Lfb_code2:");
    return;
  }

  STAMP(0);
  {
    const char* pc;
    int lo_, hi_;
    asm volatile("s_getpc_b64 %0\n.Lfb_code0:\n\ts_mov_b32 %1, .Lfb_codeA-.Lfb_code0\n\ts_mov_b32 %2, .Lfb_code1-.Lfb_code0\n\t"
                 "s_max_i32 %2, %2, .Lfb_code2-.Lfb_code0\n\ts_min_i32 %1, %1, 0" : "=s"(pc), "=s"(lo_), "=s"(hi_) :: "scc");
    const int off = min((slot * 4 + w) * 1024, hi_ - lo_ - 1024);
    if ((slot * 4 + w) * 1024 < hi_ - lo_) glds1((unsigned)lane * 16, pc + lo_ + off, sb + L_JUNK);
  }
  if (w == 0) touch4(p.ws + WS_KV + myblk * 16384, p.ws + WS_KV + (tt >= 4 ? myblk - 32 : myblk) * 16384, p.ws + WS_O + tt * 65536, p.out + (size_t)tt * 65536);
  if (w == 1) touch4(p.ws + WS_STATS + tt * 8192, p.ws + WS_HROWS + tt * 32768, p.ws + WS_KEYS, p.wo + (size_t)slot * 16384);
  if (w == 2) touch4(p.wg + (size_t)slot * 44032, p.wu + (size_t)slot * 44032, p.wd + (size_t)slot * 44032, p.ws + WS_KV + (tt >= 2 ? myblk - 16 : myblk) * 16384);
  if (w == 3) glds1((unsigned)lane * 16, p.wr, sb + L_WR);
  if (tid < 4) LDS_U1(sb + L_FLG + tid * 4) = 0u;
  const unsigned tag = read_tag(p.ws);
  const __amdgpu_buffer_rsrc_t rws = __builtin_amdgcn_make_buffer_rsrc(p.ws, 0, WS_BYTES, 0x00020000);
  const int tb = w & 1, ct = w >> 1;
  unsigned so_kv = WS_KV + myblk * 16384;
  SPIN_(so_kv);
  unsigned xa[4], wa[4], wb[4];
#pragma unroll
  for (int i = 0; i < 4; ++i) {
    const int ks = i ^ (ct | (tb << 1));
    xa[i] = sb + L_X + (tb * 32 + r32) * 128 + (((2 * ks + h) ^ ((r32 >> 1) & 7)) << 4);
    wa[i] = sb + L_RING + (16 * ks + 8 * h + q4) * 128 + ((ct ^ ((q4 >> 1) & 1)) << 6) + (G & 1) * 32 + p4 * 8;
    wb[i] = wa[i] + 49152;
    PIN(xa[i]); PIN(wa[i]); PIN(wb[i]);
  }
  unsigned a_csw = sb + L_CS + h * 512 + w * 128 + r32 * 4, a_xsw = sb + L_XS + w * 256 + r32 * 8 + h * 4;
  unsigned a_xsr = sb + L_XS + tb * 256 + r32 * 8, a_csr = sb + L_CS + ct * 256 + h * 16, a_cvr = sb + L_CS + 512 + ct * 256 + r32 * 4;
  unsigned a_tabw = sb + L_TAB + w * 256 + lane * 4, a_tabr = sb + L_TAB + w * 256 + h * 16, a_cqw = sb + L_CS + 1024 + w * 128 + r32 * 4;
  unsigned ko = tb * 8192 + 2 * ct * 1024 + lane * 16;
  unsigned vo = tb * 8192 + 4096 + (2 * ct + (r32 >> 4)) * 1024 + (h * 32 + r16) * 16;
  unsigned a_kx = sb + L_KVX + ko, a_vx = sb + L_KVX + vo, a_qw = sb + L_QX + (tb * 4 + 2 * ct) * 1024 + lane * 16;
  unsigned a_xres = sb + L_X + ((gg - rot) & 7) * 8192 + (w * 16 + r16) * 128 + (((G >> 1) ^ ((r16 >> 1) & 7)) << 4) + (G & 1) * 8;
  PIN(a_csw); PIN(a_xsw); PIN(a_xsr); PIN(a_csr); PIN(a_cvr); PIN(a_tabw); PIN(a_tabr); PIN(a_cqw);
  PIN(ko); PIN(vo); PIN(a_kx); PIN(a_vx); PIN(a_qw); PIN(a_xres);
  f32x16 acc0, acc1;
#pragma unroll
  for (int i = 0; i < 16; ++i) { acc0[i] = 0.f; acc1[i] = 0.f; }
  float cs0 = 0.f, cs1 = 0.f, xs = 0.f, xq = 0.f;
  auto rd = [&](Frags& f, int slab, int sl) {
#pragma unroll
    for (int i = 0; i < 4; ++i) {
      const unsigned wo = sl < 3 ? wa[i] + sl * 16384 : wb[i] + (sl - 3) * 16384;
      f.x[i] = LDS_U4(xa[i] + slab * 8192); f.a[i] = tr16x2(wo, 512); f.b[i] = tr16x2(wo + 8192, 512);
    }
  };
  auto sums = [&](const Frags& f, bool withx) {
    PIN(acc0);
    if (withx) PIN(acc1);
#pragma unroll
    for (int i = 0; i < 8; ++i) { __builtin_amdgcn_sched_group_barrier(0x8, 1, 0); __builtin_amdgcn_sched_group_barrier(0x100, 3, 0); }
    SB();
#pragma unroll
    for (int j = 0; j < 4; ++j) {
      cs0 = dot2(f.a[0][j], ONE2, cs0); cs1 = dot2(f.b[0][j], ONE2, cs1);
      if (withx) { xs = dot2(f.x[0][j], ONE2, xs); xq = dot2(f.x[0][j], f.x[0][j], xq); }
    }
#pragma unroll
    for (int j = 0; j < 4; ++j) {
      cs0 = dot2(f.a[1][j], ONE2, cs0); cs1 = dot2(f.b[1][j], ONE2, cs1);
      if (withx) { xs = dot2(f.x[2][j], ONE2, xs); xq = dot2(f.x[2][j], f.x[2][j], xq); }
    }
    PIN(cs0); PIN(cs1);
    if (withx) { PIN(xs); PIN(xq); }
    SB();
  };
  auto mmA = [&](const Frags& f) {
#pragma unroll
    for (int i = 0; i < 4; ++i) { acc0 = mfma32(f.a[i], f.x[i], acc0); acc1 = mfma32(f.x[i], f.b[i], acc1); }
  };
  Frags fa, fb2;
  PIN(acc0); PIN(acc1);
  STAMP2(32); BAR(); rd(fa, 0, 0); sums(fa, true);
  STAMP(20);
  STAMP2(33); BAR(); rd(fb2, 1, 1); mmA(fa); sums(fb2, true);
  STAMP2(34); BAR(); rd(fa, 2, 2); mmA(fb2); sums(fa, true);
  STAMP2(35); BAR(); rd(fb2, 3, 3); mmA(fa); sums(fb2, true);
  STAMP2(36); BAR(); rd(fa, 4, 4); mmA(fb2); sums(fa, true);
  STAMP2(37); BAR(); rd(fb2, 5, 0); mmA(fa); sums(fb2, true);
  STAMP2(38); BAR(); rd(fa, 6, 1); mmA(fb2); sums(fa, true);
  STAMP2(39); BAR(); rd(fb2, 7, 2); mmA(fa); sums(fb2, true);
  {
    auto rc = __builtin_amdgcn_permlane32_swap(asu(cs0), asu(cs1), false, false);
    auto rx = __builtin_amdgcn_permlane32_swap(asu(xs), asu(xq), false, false);
    const unsigned c0 = rc[0], c1 = rc[1], x0 = rx[0], x1 = rx[1];
    LDS_F1(a_csw) = asf(c0) + asf(c1);
    LDS_F1(a_xsw) = asf(x0) + asf(x1);
  }
  cs0 = 0.f; cs1 = 0.f;
  STAMP(27);
  BAR();
  float rs1, mr1;
  v4u xh[4];
  auto rdx = [&](int slab) {
#pragma unroll
    for (int i = 0; i < 4; ++i) xh[i] = LDS_U4(xa[i] + slab * 8192);
  };
  {
    const v2u t0 = LDS_U2(a_xsr), t1 = LDS_U2(a_xsr + 512);
    f32x4 ca[4], cb[4];
#pragma unroll
    for (int g = 0; g < 4; ++g) { ca[g] = LDS_F4(a_csr + g * 32); cb[g] = LDS_F4(a_csr + 128 + g * 32); }
    const float cv0 = LDS_F1(a_cvr), cv1 = LDS_F1(a_cvr + 128);
    SB();
#pragma unroll
    for (int i = 0; i < 4; ++i) acc0 = mfma32(fb2.a[i], fb2.x[i], acc0);
    PIN(acc0);
    float mb;
    ln_scalars(asf(t0.x) + asf(t1.x), asf(t0.y) + asf(t1.y), mb, rs1);
    mr1 = -mb * rs1;
    LDS_F1(a_tabw) = h ? mr1 : rs1;
    f32x4 r4[4], m4[4];
#pragma unroll
    for (int g = 0; g < 4; ++g) { r4[g] = LDS_F4(a_tabr + g * 32); m4[g] = LDS_F4(a_tabr + 128 + g * 32); }
    SB();
#pragma unroll
    for (int i = 0; i < 4; ++i) acc1 = mfma32(fb2.x[i], fb2.b[i], acc1);
    PIN(acc1);
    float o[16];
#pragma unroll
    for (int g = 0; g < 4; ++g)
#pragma unroll
      for (int j = 0; j < 4; ++j) o[4 * g + j] = fmaf(acc0[4 * g + j], rs1, mr1 * (ca[g][j] + cb[g][j]));
    const v4u k0 = pack8(o), k1 = pack8(o + 8);
#pragma unroll
    for (int i = 0; i < 4; ++i) { __builtin_amdgcn_sched_group_barrier(0x8, 1, 0); __builtin_amdgcn_sched_group_barrier(0x2, 6, 0); }
    SB();
    __builtin_amdgcn_raw_buffer_store_b128(k0, rws, ko, so_kv, AUX_SC1);
    __builtin_amdgcn_raw_buffer_store_b128(k1, rws, ko + 1024, so_kv, AUX_SC1);
    SB();
    const float cv = cv0 + cv1;
#pragma unroll
    for (int g = 0; g < 4; ++g)
#pragma unroll
      for (int j = 0; j < 4; ++j) o[4 * g + j] = fmaf(acc1[4 * g + j], r4[g][j], m4[g][j] * cv);
    const v4u v0 = pack8(o), v1 = pack8(o + 8);
    __builtin_amdgcn_raw_buffer_store_b128(v0, rws, vo, so_kv, AUX_SC1);
    __builtin_amdgcn_raw_buffer_store_b128(v1, rws, vo + 256, so_kv, AUX_SC1);
    SB();
    STAMP(1);
    RBAR();
    rd(fa, 0, 3); rdx(1);
    LDS_U4(a_kx) = k0; LDS_U4(a_kx + 1024) = k1;
    LDS_U4(a_vx) = v0; LDS_U4(a_vx + 256) = v1;
  }
#pragma unroll
  for (int i = 0; i < 16; ++i) acc0[i] = 0.f;
  auto mmB = [&](const Frags& f) {
#pragma unroll
    for (int i = 0; i < 4; ++i) acc0 = mfma32(f.b[i], xh[i], acc0);
#pragma unroll
    for (int i = 0; i < 4; ++i) acc0 = mfma32(f.a[i], f.x[i], acc0);
  };
  sums(fa, false);
  rd(fb2, 2, 4); mmB(fa); rdx(3); sums(fb2, false);
  WAIT_VM(0);
  BAR();
  SB();
  STAMP(2);
  rd(fa, 4, 0); mmB(fb2); rdx(5); sums(fa, false);
  rd(fb2, 6, 1); mmB(fa); rdx(7);
  v2u xres[4];
#pragma unroll
  for (int c = 0; c < 4; ++c) xres[c] = LDS_U2(a_xres ^ (c * 32));
  sums(fb2, false);
  cs0 = sum32(cs0 + cs1);
  LDS_F1(a_cqw) = cs0;
  mmB(fb2);
  PIN(acc0);
  BAR();
  {
    f32x4 ca[4], cb[4];
#pragma unroll
    for (int g = 0; g < 4; ++g) { ca[g] = LDS_F4(a_csr + 1024 + g * 32); cb[g] = LDS_F4(a_csr + 1024 + 128 + g * 32); }
    SB();
    float o[16];
    const float sc = 0.25f * 1.4426950408889634f;
#pragma unroll
    for (int g = 0; g < 4; ++g)
#pragma unroll
      for (int j = 0; j < 4; ++j) o[4 * g + j] = fmaf(acc0[4 * g + j], rs1, mr1 * (ca[g][j] + cb[g][j]));
    const v4u a0 = pack8(o), a1 = pack8(o + 8);
#pragma unroll
    for (int j = 0; j < 4; ++j) { o[2 * j] = bflo(a0[j]) * sc; o[2 * j + 1] = bfhi(a0[j]) * sc; o[8 + 2 * j] = bflo(a1[j]) * sc; o[9 + 2 * j] = bfhi(a1[j]) * sc; }
    LDS_U4(a_qw) = pack8(o);
    LDS_U4(a_qw + 1024) = pack8(o + 8);
  }
  BAR();
  STAMP(3);
  const int tok = tt * 64 + w * 16 + r16;
  unsigned so_ofr = WS_OFLAG + (tt * 2 + (w >> 1)) * 1024;
  unsigned so_st = WS_STATS + (tt * 4 + w) * 2048, so_hr = WS_HROWS + (tt * 4 + w) * 8192 + gg * 512;
  unsigned oq[4];
#pragma unroll
  for (int qn = 0; qn < 4; ++qn) { oq[qn] = WS_O + (tt * 4 + w) * 16384 + qn * 4096; SPIN_(oq[qn]); }
  SPIN_(so_ofr); SPIN_(so_st); SPIN_(so_hr);
  unsigned woa[4], wob[4];
#pragma unroll
  for (int c = 0; c < 4; ++c) {
    woa[c] = sb + L_RING + (16 * (G >> 1) + 4 * (G & 1) + q4) * 128 + ((c ^ (2 * (G & 1) + (q4 >> 1))) << 5) + p4 * 8;
    wob[c] = woa[c] + 49152;
    PIN(woa[c]); PIN(wob[c]);
  }
  auto wofrag = [&](int qn, int ks, int c) { return tr16x2((qn < 2 ? wob[c] + qn * 16384 : woa[c] + (qn - 2) * 16384) + ks * 4096, 1024); };
  v2u wr[4];
#pragma unroll
  for (int c = 0; c < 4; ++c) { wr[c] = LDS_U2(sb + L_WR + gg * 128 + G * 8 + c * 32); PIN(wr[c]); }
  const v4u wrall = LDS_U4(sb + L_WR + lane * 16);
  float wrsum;
  {
    float s = 0.f;
#pragma unroll
    for (int j = 0; j < 4; ++j) s = dot2(wrall[j], ONE2, s);
    wrsum = sum32(sum16(fsum_row16(s)));
    PIN(wrsum);
  }
  const __amdgpu_buffer_rsrc_t rout = __builtin_amdgcn_make_buffer_rsrc(p.out, 0, 2048 * 1024, 0x00020000);
  const unsigned wpriv = sb + L_PRIV + w * 2048;
  unsigned l16 = lane * 16, ofo = (lane & 7) * 128, sto = lane * 16 + gg * 256;
  unsigned key, a_cw = wpriv + G * 16, a_cr = wpriv + lane * 8, hro = h * 4096 + r32 * 16, keylo = 2047 - tok;
  unsigned fv = f2 >= 0 ? (unsigned)((lane >> 1) * 1024 + (lane & 1) * 16) : (unsigned)lane * (FF * 2);
  PIN(l16); PIN(ofo); PIN(sto); PIN(a_cw); PIN(a_cr); PIN(hro); PIN(keylo); PIN(fv);
  SB();
  RBAR();
  issue_wo(2); issue_wo(3);
  if (f1 < 172) {
#pragma unroll 1
    for (int i = w * 2; i < w * 2 + 2; ++i) {
      glds1(fv, p.wg + f1 * 16 + (size_t)i * 64 * FF * 2, sb + L_FFW + i * 1024);
      wait_vm<THR1>();
      glds1(fv, p.wu + f1 * 16 + (size_t)i * 64 * FF * 2, sb + L_FFW + 8192 + i * 1024);
      wait_vm<THR1>();
    }
  } else if (f2 >= 0) {
#pragma unroll 1
    for (int i = w; i < 43; i += 4) {
      glds1(fv, p.wd + f2 * 32 + (size_t)i * 32 * 1024, sb + L_FFW + i * 1024);
      wait_vm<THR2>();
    }
  }
  STAMP(21);
  v4u wf[2][4][4];
#pragma unroll
  for (int qn = 0; qn < 2; ++qn)
#pragma unroll
    for (int ks = 0; ks < 4; ++ks)
#pragma unroll
      for (int c = 0; c < 4; ++c) wf[qn][ks][c] = wofrag(qn, ks, c);
#pragma unroll
  for (int qn = 0; qn < 2; ++qn)
#pragma unroll
    for (int ks = 0; ks < 4; ++ks)
#pragma unroll
      for (int c = 0; c < 4; ++c) PIN(wf[qn][ks][c]);
  STAMP(22);
  WAIT_VM(0);
  lds_arrive(sb + L_FLG, lane);
  STAMP(5);
  lds_wait4(sb + L_FLG);
  float hv[16];
  {
    lds_wait4(sb + L_FLG + 8 + (w >> 1) * 4);
    __builtin_amdgcn_s_sleep(OSLEEP);
    for (unsigned s = 0; s < SPIN_CAP; ++s) {
      asm volatile("" ::: "memory");
      const unsigned f = __builtin_amdgcn_raw_buffer_load_b32(rws, ofo, so_ofr, AUX_SC1);
      if (__all(f == tag)) break;
      __builtin_amdgcn_s_sleep(2);
    }
    asm volatile("" ::: "memory");
    STAMP(9);
    v4u of[4][4];
#pragma unroll
    for (int qn = 0; qn < 4; ++qn)
#pragma unroll
      for (int ks = 0; ks < 4; ++ks) of[qn][ks] = __builtin_amdgcn_raw_buffer_load_b128(rws, l16 + ks * 1024, oq[qn], AUX_SC1);
    asm volatile("" ::: "memory");
    SB();
    f32x4 a[4];
#pragma unroll
    for (int c = 0; c < 4; ++c) a[c] = (f32x4){0.f, 0.f, 0.f, 0.f};
#pragma unroll
    for (int qn = 0; qn < 2; ++qn)
#pragma unroll
      for (int ks = 0; ks < 4; ++ks)
#pragma unroll
        for (int c = 0; c < 4; ++c) {
          a[c] = mfma16(wf[qn][ks][c], of[qn][ks], a[c]);
          wf[qn][ks][c] = wofrag(qn + 2, ks, c);
        }
#pragma unroll
    for (int c = 0; c < 4; ++c) PIN(a[c]);
#pragma unroll
    for (int i = 0; i < 32; ++i) { __builtin_amdgcn_sched_group_barrier(0x8, 1, 0); __builtin_amdgcn_sched_group_barrier(0x100, 2, 0); }
    SB();
#pragma unroll
    for (int qn = 0; qn < 2; ++qn)
#pragma unroll
      for (int ks = 0; ks < 4; ++ks)
#pragma unroll
        for (int c = 0; c < 4; ++c) a[c] = mfma16(wf[qn][ks][c], of[qn + 2][ks], a[c]);
    unsigned hb[8];
#pragma unroll
    for (int c = 0; c < 4; ++c)
#pragma unroll
      for (int m = 0; m < 2; ++m) {
        const unsigned xr = xres[c][m];
        const unsigned ab = pk2(a[c][2 * m], a[c][2 * m + 1]);
        hb[2 * c + m] = pk2(bflo(xr) + bflo(ab), bfhi(xr) + bfhi(ab));
      }
    STAMP(11);
    float s1 = 0.f, s2 = 0.f, s3 = 0.f;
#pragma unroll
    for (int m = 0; m < 8; ++m) { s1 = dot2(hb[m], ONE2, s1); s2 = dot2(hb[m], hb[m], s2); s3 = dot2(hb[m], wr[m >> 1][m & 1], s3); }
    s1 = sum32(sum16(s1)); s2 = sum32(sum16(s2)); s3 = sum32(sum16(s3));
    if (lane < 16)
      __builtin_amdgcn_raw_buffer_store_b128((v4u){asu(s1), asu(s2), asu(s3), tag ^ asu(s1) ^ asu(s2) ^ asu(s3)}, rws, sto, so_st, AUX_SC1);
    SB();
#pragma unroll
    for (int m = 0; m < 8; ++m) { hv[2 * m] = bflo(hb[m]); hv[2 * m + 1] = bfhi(hb[m]); PIN(hv[2 * m]); PIN(hv[2 * m + 1]); }
  }
  STAMP(12);
  v4u out0, out1;
  {
    float s1, s2, s3;
    for (unsigned s = 0;; ++s) {
      asm volatile("" ::: "memory");
      const v4u a = __builtin_amdgcn_raw_buffer_load_b128(rws, l16, so_st, AUX_SC1);
      const v4u c = __builtin_amdgcn_raw_buffer_load_b128(rws, l16 + 1024, so_st, AUX_SC1);
      s1 = asf(a.x) + asf(c.x); s2 = asf(a.y) + asf(c.y); s3 = asf(a.z) + asf(c.z);
      if (__all((a.x ^ a.y ^ a.z ^ a.w) == tag && (c.x ^ c.y ^ c.z ^ c.w) == tag) || s >= SPIN_CAP) break;
      __builtin_amdgcn_s_sleep(1);
    }
    STAMP(13);
    s1 = sum32(sum16(s1)); s2 = sum32(sum16(s2)); s3 = sum32(sum16(s3));
    float mb, rs;
    ln_scalars(s1, s2, mb, rs);
    const unsigned rb = pk2((s3 - mb * wrsum) * rs, 0.f) & 0xffffu;
    const unsigned ord = (rb & 0x8000u) ? (~rb & 0xffffu) : (rb | 0x8000u);
    key = (ord << 11) | keylo;
    const unsigned k1 = umax_row16(key);
    const unsigned k2 = umax_row16(key == k1 ? 0u : key);
    if (lane == 0) LDS_U2(sb + L_KEY + w * 8) = (v2u){k1, k2};
    BAR();
    unsigned hn[8];
#pragma unroll
    for (int m = 0; m < 8; ++m) {
      const unsigned d = pk2(hv[2 * m] - mb, hv[2 * m + 1] - mb);
      hn[m] = pk2(bflo(d) * rs, bfhi(d) * rs);
    }
    if (key == k1 || key == k2) {
      const unsigned base = a_cw + (key == k1 ? 0 : 256);
#pragma unroll
      for (int q = 0; q < 4; ++q) {
        const unsigned a0 = hn[2 * q], a1 = hn[2 * q + 1];
        LDS_U4(base + q * 64) = (v4u){(a0 & 0xffffu) | asu(hv[4 * q]), (a0 >> 16) | asu(hv[4 * q + 1]),
                                      (a1 & 0xffffu) | asu(hv[4 * q + 2]), (a1 >> 16) | asu(hv[4 * q + 3])};
      }
    }
    WAIT_LGKM();
    {
      const v2u e = LDS_U2(a_cr);
      __builtin_amdgcn_raw_buffer_store_b128((v4u){tag, e.x, tag, e.y}, rws, hro, so_hr, AUX_SC1);
    }
    SB();
    STAMP(14);
    unsigned ob[8];
#pragma unroll
    for (int m = 0; m < 8; ++m) ob[m] = pk2(hv[2 * m] + bflo(hn[m]), hv[2 * m + 1] + bfhi(hn[m]));
#pragma unroll
    for (int m = 0; m < 2; ++m) {
      auto r = __builtin_amdgcn_permlane16_swap(ob[m], ob[2 + m], false, false);
      auto q = __builtin_amdgcn_permlane16_swap(ob[4 + m], ob[6 + m], false, false);
      const unsigned r0 = r[0], r1 = r[1], q0 = q[0], q1 = q[1];
      out0[m] = r0; out0[2 + m] = r1; out1[m] = q0; out1[2 + m] = q1;
    }
    PIN(out0); PIN(out1);
  }
  v4u wd[11];
  if (f2 >= 0) {
    const unsigned wa2 = sb + L_FFW + (32 * w + 8 * G + q4) * 32 + p4 * 8;
#pragma unroll
    for (int i = 0; i < 11; ++i) wd[i] = tr16x2(wa2 + ((i == 10 && w == 3) ? 0 : i * 4096), 128);
    if (w == 3) wd[10] = (v4u){0u, 0u, 0u, 0u};
#pragma unroll
    for (int i = 0; i < 11; ++i) PIN(wd[i]);
  }
  RBAR();
  const v2u kk = LDS_U2(sb + L_SEL);
  const unsigned K1 = __builtin_amdgcn_readfirstlane(kk.x), K2 = __builtin_amdgcn_readfirstlane(kk.y);
  STAMP(15);
  if (key != K1 && key != K2) {
    const int col = 64 * gg + 16 * (G & 1) + 8 * (G >> 1);
    __builtin_amdgcn_raw_buffer_store_b128(out0, rout, (unsigned)(tok * 1024 + col * 2), 0, AUX_SC1);
    __builtin_amdgcn_raw_buffer_store_b128(out1, rout, (unsigned)(tok * 1024 + col * 2 + 64), 0, AUX_SC1);
  }
  if (f2 >= 0) {
    const unsigned t1 = 2047u - (K1 & 2047u), t2 = 2047u - (K2 & 2047u);
    const unsigned r1 = WS_HROWS + (t1 >> 4) * 8192, r2 = WS_HROWS + (t2 >> 4) * 8192 + ((t1 >> 4) == (t2 >> 4) ? 4096 : 0);
    v2u hg = {0u, 0u};
    float sg = 0.f;
    if (w == 0) {
      for (unsigned s = 0; s < SPIN_CAP; ++s) {
        asm volatile("" ::: "memory");
        hg = __builtin_amdgcn_raw_buffer_load_b64(rws, ((G & 1) ? r2 : r1) + (16 * f2 + r16) * 8, 0, AUX_SC1);
        if (__all(hg.x == tag)) break;
      }
      const unsigned ordk = ((G & 1) ? K2 : K1) >> 11;
      const unsigned rb = (ordk & 0x8000u) ? (ordk & 0x7fffu) : (~ordk & 0xffffu);
      sg = rbf(__builtin_amdgcn_rcpf(1.f + __builtin_amdgcn_exp2f(bflo(rb) * -1.4426950408889634f)));
      PIN(sg);
    }
    const int ksC = w + 4 * (G + 8);
    unsigned oc = (ksC < 43 ? ksC : w) * 512 + q4 * 128 + p4 * 16, oab = (w + 4 * G) * 512 + q4 * 128 + p4 * 16;
    unsigned a_b = wpriv + G * 32 + (lane & 1) * 16, a_rw = sb + L_PRIV + 8192 + (w * 2 + r16) * 64 + G * 16, a_rr = sb + L_PRIV + 8192 + (G & 1) * 64 + r16 * 4;
    unsigned oout = ((G & 1) ? t2 : t1) * 1024 + (16 * f2 + r16) * 2;
    PIN(oc); PIN(oab); PIN(a_b); PIN(a_rw); PIN(a_rr); PIN(oout);
    v4u ga, gb, gc;
    __builtin_amdgcn_s_sleep(ASLEEP);
    for (unsigned s = 0;; ++s) {
      asm volatile("" ::: "memory");
      ga = __builtin_amdgcn_raw_buffer_load_b128(rws, oab, WS_AGRAN, AUX_SC1);
      gb = __builtin_amdgcn_raw_buffer_load_b128(rws, oab, WS_AGRAN + 8192, AUX_SC1);
      gc = __builtin_amdgcn_raw_buffer_load_b128(rws, oc, WS_AGRAN, AUX_SC1);
      if (__all(ga.x == tag && ga.z == tag && gb.x == tag && gb.z == tag && gc.x == tag && gc.z == tag) || s >= SPIN_CAP) break;
      __builtin_amdgcn_s_sleep(1);
    }
    STAMP(16);
    LDS_U2(a_cr) = (v2u){ga.y, ga.w};
    LDS_U2(a_cr + 512) = (v2u){gb.y, gb.w};
    LDS_U2(a_cr + 1024) = (v2u){gc.y, gc.w};
    WAIT_LGKM();
    v4u bb[11];
#pragma unroll
    for (int i = 0; i < 11; ++i) bb[i] = LDS_U4(a_b + i * 128);
    SB();
    f32x4 d0 = {0.f, 0.f, 0.f, 0.f}, d1 = d0;
#pragma unroll
    for (int i = 0; i < 11; ++i) { if (i & 1) d1 = mfma16(wd[i], bb[i], d1); else d0 = mfma16(wd[i], bb[i], d0); }
    d0 += d1;
    if (r16 < 2) LDS_F4(a_rw) = d0;
    BAR();
    if (w == 0) {
      float y = (LDS_F1(a_rr) + LDS_F1(a_rr + 128)) + (LDS_F1(a_rr + 256) + LDS_F1(a_rr + 384));
      y = rbf(rbf(y) * sg);
      const float t = rbf(bflo(hg.y) + y);
      const unsigned ov = pk2(bfhi(hg.y) + t, 0.f) & 0xffffu;
      const unsigned nb = dppu<0xB1>(ov);
      if (lane < 32 && (lane & 1) == 0) __builtin_amdgcn_raw_buffer_store_b32(ov | (nb << 16), rout, oout, 0, AUX_SC1);
    }
    STAMP(17);
  }
  STAMP(18);
  STAMP_OUT();
  asm volatile(".Lfb_code1:");
}
__global__ void tiny_kernel(int ticks) {
  const u64 t = __builtin_amdgcn_s_memrealtime();
  for (int i = 0; i < 100000; ++i) { if (__builtin_amdgcn_s_memrealtime() - t >= (u64)ticks) break; }
}
}

extern "C" void kernel_launch(void* const* d_in, const int* in_sizes, int n_in, void* d_out, int out_size, void* d_ws,
                              size_t ws_size, hipStream_t stream) {
  Params p;
  p.x = (const char*)d_in[0]; p.wqkv = (const char*)d_in[1]; p.wo = (const char*)d_in[2];
  p.wr = (const char*)d_in[4]; p.wg = (const char*)d_in[5]; p.wu = (const char*)d_in[6]; p.wd = (const char*)d_in[7];
  p.out = (char*)d_out; p.ws = (char*)d_ws;
  (void)hipFuncSetAttribute((const void*)fused_block_kernel, hipFuncAttributeMaxDynamicSharedMemorySize, L_TOTAL);
  hipLaunchKernelGGL(fused_block_kernel, dim3(256), dim3(512), L_TOTAL, stream, p);
  hipLaunchKernelGGL(tiny_kernel, dim3(1), dim3(64), 0, stream, 0);
}
```

```cpp
#include <hip/hip_runtime.h>
#include <hip/hip_bf16.h>

typedef unsigned long long u64;
typedef unsigned v4u __attribute__((ext_vector_type(4)));
typedef unsigned v2u __attribute__((ext_vector_type(2)));
typedef float f32x2 __attribute__((ext_vector_type(2)));
typedef float f32x4 __attribute__((ext_vector_type(4)));
typedef float f32x16 __attribute__((ext_vector_type(16)));
typedef __bf16 bf16x2 __attribute__((ext_vector_type(2)));
typedef __bf16 bf16x4 __attribute__((ext_vector_type(4)));
typedef __bf16 bf16x8 __attribute__((ext_vector_type(8)));
#define LAS __attribute__((address_space(3)))
#define DI __device__ __forceinline__

namespace {
constexpr int FF = 1376;
constexpr int AUX_SC1 = 16;
constexpr unsigned SPIN_CAP = 1u << 16;
constexpr unsigned WS_EPOCH = 0;
constexpr unsigned WS_KVFLAG = 4096;
constexpr unsigned WS_OFLAG = WS_KVFLAG + 256 * 128;
constexpr unsigned WS_KEYS = WS_OFLAG + 256 * 128;
constexpr unsigned WS_AGRAN = WS_KEYS + 256 * 128;
constexpr unsigned WS_STATS = 1u << 18;
constexpr unsigned WS_XREC = 2u << 18;
constexpr unsigned WS_WREC = 3u << 18;
constexpr unsigned WS_HROWS = 1u << 20;
constexpr unsigned WS_O = 2u << 20;
constexpr unsigned WS_KV = 4u << 20;
constexpr unsigned WS_BYTES = 8u << 20;
static_assert(WS_AGRAN + 192 * 128 <= WS_STATS, "ws map");
constexpr int L_X = 0;
constexpr int L_RING = 65536;
constexpr int L_MISC = L_RING + 5 * 16384;
constexpr int L_TAB = L_MISC;
constexpr int L_XS = L_TAB + 1280;
constexpr int L_CS = L_XS + 1024;
constexpr int L_KEY = L_CS + 1536;
constexpr int L_SEL = L_KEY + 64;
constexpr int L_QX = L_MISC + 4096;
constexpr int L_WR = L_QX + 8192;
constexpr int L_JUNK = L_WR + 1024;
constexpr int L_KVX = L_RING + 2 * 16384;
constexpr int L_FFW = 0;
constexpr int L_PRIV = 49152;
constexpr int L_TOTAL = 163840;
static_assert(L_SEL + 16 <= L_QX && L_JUNK + 1024 <= L_TOTAL, "lds map");

constexpr int KVSLEEP = 5, OSLEEP = 9, SSLEEP = 16, OUTEARLY = 4, PGAP = 1, ASLEEP = 35;
constexpr float FB_LIMIT = 1e18f;
struct Params {
  const char* x; const char* wqkv; const char* wo; const char* wr; const char* wg; const char* wu; const char* wd;
  char* out; char* ws;
};

DI float asf(unsigned u) { return __builtin_bit_cast(float, u); }
DI unsigned asu(float f) { return __builtin_bit_cast(unsigned, f); }
DI float bflo(unsigned u) { return asf(u << 16); }
DI float bfhi(unsigned u) { return asf(u & 0xffff0000u); }
DI unsigned pk2(float a, float b) { f32x2 f = {a, b}; return __builtin_bit_cast(unsigned, __builtin_convertvector(f, bf16x2)); }
DI float rbf(float a) { return bflo(pk2(a, a)); }
DI float dot2(unsigned a, unsigned b, float c) {
  return __builtin_amdgcn_fdot2_f32_bf16(__builtin_bit_cast(bf16x2, a), __builtin_bit_cast(bf16x2, b), c, false);
}
DI f32x16 mfma32(v4u a, v4u b, f32x16 c) {
  return __builtin_amdgcn_mfma_f32_32x32x16_bf16(__builtin_bit_cast(bf16x8, a), __builtin_bit_cast(bf16x8, b), c, 0, 0, 0);
}
DI f32x4 mfma16(v4u a, v4u b, f32x4 c) {
  return __builtin_amdgcn_mfma_f32_16x16x32_bf16(__builtin_bit_cast(bf16x8, a), __builtin_bit_cast(bf16x8, b), c, 0, 0, 0);
}
DI float sum32(float x) {
  auto r = __builtin_amdgcn_permlane32_swap(asu(x), asu(x), false, false);
  const unsigned r0 = r[0], r1 = r[1];
  return asf(r0) + asf(r1);
}
DI float sum16(float x) {
  auto r = __builtin_amdgcn_permlane16_swap(asu(x), asu(x), false, false);
  const unsigned r0 = r[0], r1 = r[1];
  return asf(r0) + asf(r1);
}
DI float fmax32(float x) {
  auto r = __builtin_amdgcn_permlane32_swap(asu(x), asu(x), false, false);
  const unsigned r0 = r[0], r1 = r[1];
  return fmaxf(asf(r0), asf(r1));
}
template <int C> DI unsigned dppu(unsigned k) { return (unsigned)__builtin_amdgcn_update_dpp(0, (int)k, C, 0xF, 0xF, true); }
template <int C> DI float dppf(float k) { return asf(dppu<C>(asu(k))); }
DI unsigned umax_row16(unsigned k) {
  k = max(k, dppu<0xB1>(k)); k = max(k, dppu<0x4E>(k)); k = max(k, dppu<0x141>(k)); k = max(k, dppu<0x140>(k));
  return k;
}
DI float fsum8(float v) { v += dppf<0xB1>(v); v += dppf<0x4E>(v); v += dppf<0x141>(v); return v; }
DI float fsum_row16(float v) { v = fsum8(v); v += dppf<0x140>(v); return v; }
DI void ln_scalars(float s1, float s2, float& mb, float& rs) {
  const float m = s1 * (1.f / 512.f);
  const float var = fmaxf(fmaf(-m, m, s2 * (1.f / 512.f)), 0.f);
  mb = rbf(m);
  rs = __builtin_amdgcn_rcpf(rbf(__builtin_amdgcn_sqrtf(rbf(rbf(var) + 1e-5f))));
}
DI void glds_chunk(unsigned v0, unsigned v1, const char* ba, const char* bb, unsigned m0v) {
  asm volatile("s_mov_b32 m0, %4\n\ts_nop 0\n\tglobal_load_lds_dwordx4 %0, %2\n\t"
               "s_add_u32 m0, m0, 0x1000\n\ts_nop 0\n\tglobal_load_lds_dwordx4 %1, %2\n\t"
               "s_add_u32 m0, m0, 0x1000\n\ts_nop 0\n\tglobal_load_lds_dwordx4 %0, %3\n\t"
               "s_add_u32 m0, m0, 0x1000\n\ts_nop 0\n\tglobal_load_lds_dwordx4 %1, %3"
               :: "v"(v0), "v"(v1), "s"(ba), "s"(bb), "s"(m0v) : "memory", "scc");
}
DI void glds2(unsigned v, const char* ba, const char* bb, unsigned m0v) {
  asm volatile("s_mov_b32 m0, %3\n\ts_nop 0\n\tglobal_load_lds_dwordx4 %0, %1\n\t"
               "s_add_u32 m0, m0, 0x1000\n\ts_nop 0\n\tglobal_load_lds_dwordx4 %0, %2"
               :: "v"(v), "s"(ba), "s"(bb), "s"(m0v) : "memory", "scc");
}
template <int N> DI void wait_vm() { asm volatile("s_waitcnt vmcnt(%0)" :: "n"(N) : "memory"); }
DI void glds1(unsigned v, const char* ba, unsigned m0v) {
  asm volatile("s_mov_b32 m0, %2\n\ts_nop 0\n\tglobal_load_lds_dwordx4 %0, %1" :: "v"(v), "s"(ba), "s"(m0v) : "memory");
}
#define WAIT_VM(n) asm volatile("s_waitcnt vmcnt(" #n ")" ::: "memory")
#define WAIT_LGKM() asm volatile("s_waitcnt lgkmcnt(0)" ::: "memory")
#define BAR() do { __builtin_amdgcn_sched_barrier(0); asm volatile("s_waitcnt lgkmcnt(0)\n\ts_barrier" ::: "memory"); __builtin_amdgcn_sched_barrier(0); } while (0)
#define PIN(x) asm volatile("" : "+v"(x))
#define SPIN_(x) asm volatile("" : "+s"(x))
#define SB() __builtin_amdgcn_sched_barrier(0)
#define LDS_U4(off) (*(LAS v4u*)(size_t)(off))
#define LDS_F4(off) (*(LAS f32x4*)(size_t)(off))
#define LDS_U2(off) (*(LAS v2u*)(size_t)(off))
#define LDS_F1(off) (*(LAS float*)(size_t)(off))
#define LDS_U1(off) (*(LAS unsigned*)(size_t)(off))
DI v2u tr16(unsigned off) { return __builtin_bit_cast(v2u, __builtin_amdgcn_ds_read_tr16_b64_v4bf16((LAS bf16x4*)(size_t)off)); }
DI v4u tr16x2(unsigned off, unsigned d) { const v2u a = tr16(off), b = tr16(off + d); return (v4u){a.x, a.y, b.x, b.y}; }
DI unsigned next_tag(unsigned e) { return max(e * 1664525u + 1013904223u, 1u); }
DI unsigned read_tag(const char* ws) {
  unsigned e;
  asm volatile("s_load_dword %0, %1, 0x0\n\ts_waitcnt lgkmcnt(0)" : "=s"(e) : "s"(ws) : "memory");
  return next_tag(e);
}
DI v4u pack8(const float* o) { return (v4u){pk2(o[0], o[1]), pk2(o[2], o[3]), pk2(o[4], o[5]), pk2(o[6], o[7])}; }

constexpr unsigned ONE2 = 0x3F803F80u;
struct Frags { v4u x[4], a[4], b[4]; };
#define FENCE_() asm volatile("" ::: "memory")
template <class T, class LD, class OK> DI T poll(LD ld, OK ok) {
  T a;
#pragma unroll 1
  for (unsigned s = 0;; ++s) {
    FENCE_();
    a = ld();
    if (__all(ok(a)) || s >= SPIN_CAP) break;
    __builtin_amdgcn_s_sleep(PGAP);
  }
  FENCE_();
  return a;
}
struct V2 { v4u a, c; };
struct V3 { v4u a, b, c; };
struct V2h { v2u x, y; };
#define RBAR() asm volatile("s_barrier" ::: "memory")

__global__ __launch_bounds__(512, 2) void fused_block_kernel(Params p) {
  extern __shared__ __attribute__((aligned(16))) char smem[];
  const int lane = threadIdx.x & 63;
  const int wv = __builtin_amdgcn_readfirstlane(threadIdx.x >> 6);
  if (wv >= 4) {
    const char* pc;
    int len;
    asm volatile("s_getpc_b64 %0\n.Lfb_code0:\n\ts_mov_b32 %1, .Lfb_code1-.Lfb_code0\n\ts_max_i32 %1, %1, .Lfb_code2-.Lfb_code0" : "=s"(pc), "=s"(len) :: "scc");
    const int off = ((blockIdx.x >> 3) * 4 + wv - 4) * 1024;
    if (off < len) glds1((unsigned)lane * 16, pc + min(off, len - 1024), (unsigned)(size_t)(LAS char*)smem + L_JUNK);
  }
  const int w = wv & 3, tid = threadIdx.x & 255;
  const int b = blockIdx.x, xcd = b & 7, slot = b >> 3;
  const int tt = (xcd & 3) * 8 + (slot & 7), gg = (xcd >> 2) * 4 + (slot >> 3);
  const unsigned sb = (unsigned)(size_t)(LAS char*)smem;
  const int rot = (tt + 2 * gg + (gg >> 2)) & 7;
  const int f1 = slot < 22 ? xcd * 22 + slot : 999;
  const int f2 = (slot >= 22 && slot < 26) ? xcd * 4 + slot - 22 : -1;
  const int r32 = lane & 31, h = lane >> 5, r16 = lane & 15, G = lane >> 4, q4 = r16 >> 2, p4 = lane & 3;
  const int wrow = tid >> 3;
  const unsigned myblk = (unsigned)(tt * 8 + gg);
  const unsigned ov0 = (unsigned)(wrow * 1024 + gg * 128 + (((tid & 7) ^ (((wrow >> 1) & 3) << 1)) << 4)), ov1 = ov0 + 32 * 1024;
  auto issue_wo = [&](int qn) {
    const char* s0 = p.wo + (size_t)(((qn + tt) & 3) * 128) * 1024;
    glds_chunk(ov0, ov1, s0, s0 + 64 * 1024, sb + L_RING + w * 1024 + ((qn + 3) % 5) * 16384);
  };

  if (__builtin_expect(wv < 4, 1)) {
    unsigned tag;
    const __amdgpu_buffer_rsrc_t rws = __builtin_amdgcn_make_buffer_rsrc(p.ws, 0, WS_BYTES, 0x00020000);
    {
      const unsigned xv = (unsigned)(wrow * 1024 + (((tid & 7) ^ ((wrow >> 1) & 7)) << 4));
      const unsigned wv0 = (unsigned)(wrow * 3072 + gg * 128 + (((tid & 7) ^ (((wrow >> 1) & 1) << 2)) << 4)), wv1 = wv0 + 32 * 3072;
      const unsigned ringw = sb + L_RING + w * 1024;
      const char* xb = p.x + (size_t)tt * 65536;
      auto issue = [&](int n) {
        if (n < 8) {
          const int c = (n + rot) & 7;
          glds2(xv, xb + c * 128, xb + c * 128 + 32768, sb + L_X + n * 8192 + w * 1024);
          const char* s0 = p.wqkv + (size_t)(c * 64) * 3072 + 1024;
          glds_chunk(wv0, wv1, s0, s0 + 1024, ringw + (n % 5) * 16384);
          if (n == 0 && w == 3) glds1((unsigned)lane * 16, p.wr, sb + L_WR);
          if (n == 0) glds2(wv0, s0 - 1024, s0 - 1024 + 32 * 3072, sb + L_QX + w * 1024);
        } else {
          glds_chunk(wv0, wv1, p.wqkv + (size_t)(((2 * n + rot) & 7) * 64) * 3072, p.wqkv + (size_t)(((2 * n + 1 + rot) & 7) * 64) * 3072,
                     ringw + (n % 5) * 16384);
        }
      };
      asm volatile("s_load_dword %0, %1, 0x0" : "=s"(tag) : "s"(p.ws) : "memory");
      issue(0); issue(1);
      unsigned pa = w < 2 ? sb + L_XS + w * 256 + r32 * 8 : sb + L_CS + (w - 2) * 256 + r32 * 4;
      unsigned pb = pa + (w < 2 ? 512 : 128);
      const unsigned dp = w < 2 ? 4 : 512;
      unsigned pa1 = pa + dp, pa2 = pa + 2 * dp, pb1 = pb + dp, pb2 = pb + 2 * dp;
      unsigned so_rec = w < 2 ? WS_XREC + tt * 8192 : WS_WREC + (gg * 4 + (tt >> 3)) * 8192;
      unsigned pv = (w < 2 ? gg : (tt & 7)) * 1024 + ((w & 1) * 32 + r32) * 16, gv = h * 4096 + ((w & 1) * 32 + r32) * 16;
      unsigned ta = sb + L_TAB + (w >> 1) * 512 + h * 256 + ((w & 1) * 32 + r32) * 4;
      PIN(pa); PIN(pb); PIN(pa1); PIN(pa2); PIN(pb1); PIN(pb2); PIN(pv); PIN(gv); PIN(ta); SPIN_(so_rec);
      float x0, x1, x2, y0, y1, y2;
      v4u g0, g1, g2, g3;
      auto PUB_READ = [&]() { x0 = LDS_F1(pa); x1 = LDS_F1(pa1); x2 = LDS_F1(pa2); y0 = LDS_F1(pb); y1 = LDS_F1(pb1); y2 = LDS_F1(pb2); SB(); };
      auto PUB_STORE = [&]() {
        asm volatile("s_waitcnt lgkmcnt(0)" : "+s"(tag) :: "memory");
        tag = next_tag(tag);
        const unsigned u0 = asu(x0 + y0), u1 = asu(x1 + y1), u2 = asu(x2 + y2);
        if (lane < 32) __builtin_amdgcn_raw_buffer_store_b128((v4u){u0, u1, u2, tag ^ u0 ^ u1 ^ u2}, rws, pv, so_rec, AUX_SC1);
      };
      auto GATHER = [&]() {
        asm volatile("global_load_dwordx4 %0, %4, %5 sc1\n\tglobal_load_dwordx4 %1, %4, %5 offset:1024 sc1\n\t"
                     "global_load_dwordx4 %2, %4, %5 offset:2048 sc1\n\tglobal_load_dwordx4 %3, %4, %5 offset:3072 sc1"
                     : "=&v"(g0), "=&v"(g1), "=&v"(g2), "=&v"(g3) : "v"(gv), "s"(p.ws + so_rec) : "memory");
      };
      auto TABLES = [&]() {
        for (unsigned s = 0; s < SPIN_CAP; ++s) {
          const unsigned bad = ((g0.x ^ g0.y ^ g0.z ^ g0.w) ^ tag) | ((g1.x ^ g1.y ^ g1.z ^ g1.w) ^ tag) | ((g2.x ^ g2.y ^ g2.z ^ g2.w) ^ tag) |
                               ((g3.x ^ g3.y ^ g3.z ^ g3.w) ^ tag);
          if (__builtin_expect(__all(bad == 0u), 1)) break;
          __builtin_amdgcn_s_sleep(2);
          GATHER();
          asm volatile("s_waitcnt vmcnt(0)" : "+v"(g0), "+v"(g1), "+v"(g2), "+v"(g3) :: "memory");
        }
        const float v0 = sum32((asf(g0.x) + asf(g1.x)) + (asf(g2.x) + asf(g3.x))), v1 = sum32((asf(g0.y) + asf(g1.y)) + (asf(g2.y) + asf(g3.y)));
        if (w < 2) {
          float mb, rs;
          ln_scalars(v0, v1, mb, rs);
          LDS_F1(ta) = h ? -mb * rs : rs;
        } else {
          const float v2 = sum32((asf(g0.z) + asf(g1.z)) + (asf(g2.z) + asf(g3.z)));
          LDS_F1(ta) = h ? v1 : v0;
          LDS_F1(ta + 512 - h * 256) = v2;
        }
      };
      wait_vm<6>(); RBAR();
      issue(2); issue(3); issue(4);
      wait_vm<18>(); RBAR();
      PUB_READ();
      issue(5);
      PUB_STORE();
      wait_vm<18>(); RBAR();
      issue(6);
      wait_vm<18>(); RBAR();
      issue(7);
      wait_vm<18>(); RBAR();
      wait_vm<12>(); RBAR();
      GATHER();
      wait_vm<6>(); RBAR();
      issue(8);
      wait_vm<4>(); RBAR();
      asm volatile("s_waitcnt vmcnt(4)" : "+v"(g0), "+v"(g1), "+v"(g2), "+v"(g3) :: "memory");
      TABLES();
      issue(9);
      wait_vm<4>(); BAR();
      issue(10); issue(11);
    }
    unsigned so_ow = WS_O + (tt * 64 + gg * 2 + (w >> 1)) * 1024, so_ofw = WS_OFLAG + myblk * 128, so_kvf = WS_KVFLAG + myblk * 128;
    unsigned sot[4];
#pragma unroll
    for (int t = 0; t < 4; ++t) { sot[t] = tt - 4 + t >= 0 ? WS_KV + (myblk - (4 - t) * 8) * 16384 : WS_BYTES; SPIN_(sot[t]); }
    SPIN_(so_ow); SPIN_(so_ofw); SPIN_(so_kvf);
    unsigned kvfo;
    {
      const int st = tt - 4 + (lane & 3);
      kvfo = WS_KVFLAG + ((st < 0 ? tt : st) * 8 + gg) * 128;
    }
    unsigned lo = w * 1024 + lane * 16, lo1 = lo + 4096, lo2 = lo + 8192, lo3 = lo + 12288;
    unsigned a_q = sb + L_QX + lo, a_kv = sb + L_KVX + lo;
    unsigned ooff0 = ((unsigned)(r32 >> 4) * 16) * 1024 + (r16 + 16 * ((w & 1) * 2 + h)) * 16, ooff1 = ooff0 + 32768;
    PIN(kvfo); PIN(lo); PIN(lo1); PIN(lo2); PIN(lo3); PIN(a_q); PIN(a_kv); PIN(ooff0); PIN(ooff1);
    u64 vis[16];
#pragma unroll
    for (int i = 0; i < 16; ++i) {
      const int c0 = (i & 3) + 8 * (i >> 2);
      vis[i] = (u64)(0xffffffffu << c0) | ((u64)(0xffffffffu << (c0 + 4)) << 32);
      SPIN_(vis[i]);
    }
    auto sel = [&](int i, float yes, float no) {
      float r;
      asm("v_cndmask_b32_e64 %0, %1, %2, %3" : "=v"(r) : "v"(no), "v"(yes), "s"(vis[i]));
      return r;
    };
    SB();
    wait_vm<8>(); RBAR();
    SB();
    unsigned pm[8];
#pragma unroll
    for (int i = 0; i < 8; ++i) {
      const int k0 = ((2 * i) & 3) + 8 * (i >> 1) + 4 * h;
      pm[i] = (k0 <= r32 ? 0xffffu : 0u) | (k0 + 1 <= r32 ? 0xffff0000u : 0u);
      PIN(pm[i]);
    }
    SB();
    WAIT_VM(0); RBAR();
    if (threadIdx.x < 8) __builtin_amdgcn_raw_buffer_store_b128((v4u){tag, tag, tag, tag}, rws, lo, so_kvf, AUX_SC1);
    SB();
    const int first = 8 - 2 * tt;
    f32x16 cz;
    auto fill = [&](f32x16& blk, float v) {
      f32x2 v2 = {v, v};
      PIN(v2);
#pragma unroll
      for (int i = 0; i < 16; i += 2) { blk[i] = v2[0]; blk[i + 1] = v2[1]; }
      PIN(blk);
    };
    v4u kf[10], va[10], vb[10], qf[2];
#pragma unroll
    for (int k = 0; k < 10; ++k) { const unsigned one = k >= first ? ONE2 : 0u; vb[k] = (v4u){one, one, one, one}; PIN(vb[k]); }
    kf[8] = LDS_U4(a_kv); kf[9] = LDS_U4(a_kv + 8192);
    va[8] = LDS_U4(a_kv + 4096); va[9] = LDS_U4(a_kv + 12288);
    auto vfix = [&](int k) {
#pragma unroll
      for (int j = 0; j < 4; ++j) {
        auto r = __builtin_amdgcn_permlane16_swap(va[k][j], vb[k][j], false, false);
        const unsigned r0 = r[0], r1 = r[1];
        va[k][j] = r0; vb[k][j] = r1;
      }
      PIN(va[k]); PIN(vb[k]);
    };
    vfix(8); vfix(9);
    SB();
    __builtin_amdgcn_s_sleep(KVSLEEP);
    poll<unsigned>([&]() { return __builtin_amdgcn_raw_buffer_load_b32(rws, kvfo, 0, AUX_SC1); }, [&](unsigned f) { return f == tag; });
    auto ldk = [&](int k) { kf[k] = __builtin_amdgcn_raw_buffer_load_b128(rws, (k & 1) ? lo2 : lo, sot[k >> 1], AUX_SC1); SB(); };
    auto ldv = [&](int k) { va[k] = __builtin_amdgcn_raw_buffer_load_b128(rws, (k & 1) ? lo3 : lo1, sot[k >> 1], AUX_SC1); SB(); };
    ldk(0); ldk(1); ldv(0); ldv(1); ldk(2); ldk(3);
    RBAR();
    qf[0] = LDS_U4(a_q); qf[1] = LDS_U4(a_q + 4096);
    SB();
    ldv(2); ldv(3); ldk(4);
    f32x16 zero;
#pragma unroll
    for (int i = 0; i < 16; ++i) zero[i] = 0.f;
    auto mx16 = [&](float& ma, float& mb, const f32x16& s) {
#pragma unroll
      for (int i = 0; i < 16; i += 4) { ma = fmaxf(fmaxf(ma, s[i]), s[i + 1]); mb = fmaxf(fmaxf(mb, s[i + 2]), s[i + 3]); }
      PIN(ma); PIN(mb);
    };
    {
      float m0 = 0.f, m1 = 0.f;
#pragma unroll
      for (int j = 0; j < 4; ++j) { m0 = dot2(qf[0][j], kf[8][j], m0); m1 = dot2(qf[1][j], kf[9][j], m1); }
      m0 = sum32(m0); m1 = sum32(m1);
      PIN(m0); PIN(m1);
      SB();
      ldk(5); ldv(4);
      f32x16 sd, s0, s1;
      fill(cz, -m0);
      SB();
      ldv(5); ldk(6);
      sd = mfma32(kf[8], qf[0], cz); PIN(sd);
      SB();
      ldk(7); ldv(6); ldv(7);
      RBAR();
      float neg = -1e30f;
      PIN(neg);
      auto norm = [&](const f32x16& o) {
        const float inv = __builtin_amdgcn_rcpf(o[8]);
        float on[8];
#pragma unroll
        for (int i = 0; i < 8; ++i) on[i] = o[i] * inv;
        return pack8(on);
      };
      auto guard = [&](const f32x16& o) {
        const f32x2 t = ((f32x2){o[0], o[1]} + (f32x2){o[2], o[3]}) + ((f32x2){o[4], o[5]} + (f32x2){o[6], o[7]});
        return fmaf(t[0] + t[1], 0.f, o[8]);
      };
      f32x16 oa;
      v4u o0;
      float l0;
      auto pass = [&](int qb, float mx, bool redo) __attribute__((always_inline)) {
        f32x16 o;
        v4u pp;
        if (redo || qb == 1) { fill(cz, -mx); SB(); sd = mfma32(kf[8 + qb], qf[qb], cz); PIN(sd); }
        if (redo && first > 0) fill(cz, -1e30f);
        s1 = mfma32(kf[qb], qf[qb], cz); PIN(s1);
        SB();
        if (!redo && qb == 0) { vfix(0); vfix(1); asm volatile("s_nop 2"); }
        else if (!redo) { o0 = norm(oa); l0 = guard(oa); PIN(o0); PIN(l0); }
        else asm volatile("s_nop 7\n\ts_nop 3");
        SB();
        if (redo && qb == 1 && first == 2) fill(cz, -mx);
        s0 = mfma32(kf[1 + qb], qf[qb], cz); PIN(s0);
        SB();
        {
          float e[16];
#pragma unroll
          for (int i = 0; i < 16; ++i) e[i] = __builtin_amdgcn_exp2f(sel(i, sd[i], s1[i]));
          const v4u p0 = pack8(e), p1 = pack8(e + 8);
          v4u d0, d1, f0, f1_;
#pragma unroll
          for (int j = 0; j < 4; ++j) { d0[j] = p0[j] & pm[j]; f0[j] = p0[j] ^ d0[j]; d1[j] = p1[j] & pm[4 + j]; f1_[j] = p1[j] ^ d1[j]; }
          o = mfma32(va[8 + qb], d0, zero);
          o = mfma32(va[qb], f0, o);
          o = mfma32(vb[8 + qb], d1, o);
          pp = f1_;
          PIN(pp); PIN(o);
        }
        SB();
#pragma unroll
        for (int k = 1 + qb; k < 8 + qb; ++k) {
          float e[16];
          if (redo && k + 1 < 8 + qb && (k & 1) == 1 && first == k + 1) fill(cz, -mx);
#pragma unroll
          for (int i = 0; i < 4; ++i) e[i] = __builtin_amdgcn_exp2f(s0[i]);
          SB();
          o = mfma32(vb[k - 1], pp, o);
          PIN(o);
          SB();
#pragma unroll
          for (int i = 4; i < 8; ++i) e[i] = __builtin_amdgcn_exp2f(s0[i]);
          SB();
          if (k + 1 < 8 + qb) { s1 = mfma32(kf[k + 1], qf[qb], cz); PIN(s1); }
          SB();
          v4u p0 = pack8(e);
          PIN(p0);
          SB();
          e[8] = __builtin_amdgcn_exp2f(s0[8]); e[9] = __builtin_amdgcn_exp2f(s0[9]);
          SB();
          o = mfma32(va[k], p0, o);
          PIN(o);
          SB();
#pragma unroll
          for (int i = 10; i < 16; ++i) e[i] = __builtin_amdgcn_exp2f(s0[i]);
          pp = pack8(e + 8);
          PIN(pp);
          if (!redo && qb == 0 && k + 1 < 8) vfix(k + 1);
          SB();
          s0 = s1;
        }
        return mfma32(vb[7 + qb], pp, o);
      };
      auto again = [&](int qb) __attribute__((always_inline)) {
        float ta = -3e38f, tb_ = -3e38f;
        {
          f32x16 td = mfma32(kf[8 + qb], qf[qb], zero), tf = mfma32(kf[qb], qf[qb], zero);
          PIN(td); PIN(tf);
          asm volatile("s_nop 7\n\ts_nop 3");
          SB();
#pragma unroll
          for (int i = 0; i < 16; ++i) td[i] = sel(i, td[i], first > 0 ? neg : tf[i]);
          mx16(ta, tb_, td);
        }
#pragma unroll
        for (int k = 1 + qb; k < 8 + qb; ++k)
          if (k >= first) mx16(ta, tb_, mfma32(kf[k], qf[qb], zero));
        return norm(pass(qb, fmax32(fmaxf(ta, tb_)), true));
      };
      oa = pass(0, m0, false);
      PIN(oa);
      const f32x16 ob = pass(1, m1, false);
      v4u o1 = norm(ob);
      const float l1 = guard(ob);
      if (__builtin_expect(!__all(l0 < FB_LIMIT && l1 < FB_LIMIT && l0 > 1e-6f && l1 > 1e-6f), 0)) { o0 = again(0); o1 = again(1); }
      __builtin_amdgcn_raw_buffer_store_b128(o0, rws, ooff0, so_ow, AUX_SC1);
      __builtin_amdgcn_raw_buffer_store_b128(o1, rws, ooff1, so_ow, AUX_SC1);
    }
    WAIT_VM(0); RBAR();
    if (threadIdx.x < 8) __builtin_amdgcn_raw_buffer_store_b128((v4u){tag, tag, tag, tag}, rws, lo, so_ofw, AUX_SC1);
    if (w != 0) return;
    unsigned so_keyr = WS_KEYS + xcd * 4096, so_key = WS_KEYS + tt * 128, kro = r32 * 128, kwo = (lane >> 3) * 4096 + (lane & 7) * 16;
    unsigned l16 = lane * 16, st_w = sb + L_PRIV + 8192 + lane * 8, st_r = sb + L_PRIV + 8192 + (lane & 1) * 1024 + G * 16;
    SPIN_(so_keyr); SPIN_(so_key); PIN(kro); PIN(kwo); PIN(l16); PIN(st_w); PIN(st_r);
    RBAR();
    if (gg == 0) {
      const unsigned kq = LDS_U1(sb + L_KEY + (lane & 7) * 4);
      unsigned k1 = max(kq, dppu<0xB1>(kq)); k1 = max(k1, dppu<0x4E>(k1)); k1 = max(k1, dppu<0x141>(k1));
      unsigned k2 = kq == k1 ? 0u : kq; k2 = max(k2, dppu<0xB1>(k2)); k2 = max(k2, dppu<0x4E>(k2)); k2 = max(k2, dppu<0x141>(k2));
      __builtin_amdgcn_raw_buffer_store_b128((v4u){tag, k1, tag, k2}, rws, kwo, so_key, AUX_SC1);
    }
    v4u a[16];
    if (f1 < 172) {
      const unsigned wa = sb + L_FFW + (p4 >> 1) * 8192 + (8 * G + q4) * 16 + (p4 & 1) * 8;
#pragma unroll
      for (int i = 0; i < 16; ++i) a[i] = tr16x2(wa + i * 512, 64);
#pragma unroll
      for (int i = 0; i < 16; ++i) PIN(a[i]);
    }
    unsigned K1, K2, r1, r2;
    v4u g[4];
    {
      const v4u ka = poll<v4u>([&]() { return __builtin_amdgcn_raw_buffer_load_b128(rws, kro, so_keyr, AUX_SC1); },
                               [&](const v4u& k) { return k.x == tag && k.z == tag; });
      auto rmax2 = [&](unsigned v) { return max((unsigned)__builtin_amdgcn_readlane(v, 0), (unsigned)__builtin_amdgcn_readlane(v, 16)); };
      K1 = rmax2(umax_row16(ka.y));
      const unsigned g1 = (2047u - (K1 & 2047u)) >> 4;
      r1 = WS_HROWS + g1 * 8192;
      g[0] = __builtin_amdgcn_raw_buffer_load_b128(rws, l16, r1, AUX_SC1); g[1] = __builtin_amdgcn_raw_buffer_load_b128(rws, l16 + 1024, r1, AUX_SC1);
      FENCE_();
      SB();
      K2 = rmax2(umax_row16(ka.y == K1 ? ka.w : ka.y));
      const unsigned g2 = (2047u - (K2 & 2047u)) >> 4;
      r2 = WS_HROWS + g2 * 8192 + (g1 == g2 ? 4096 : 0);
      g[2] = __builtin_amdgcn_raw_buffer_load_b128(rws, l16, r2, AUX_SC1); g[3] = __builtin_amdgcn_raw_buffer_load_b128(rws, l16 + 1024, r2, AUX_SC1);
      FENCE_();
      SB();
    }
    if (lane == 0) LDS_U2(sb + L_SEL) = (v2u){K1, K2};
    asm volatile("s_waitcnt lgkmcnt(0)\n\ts_barrier" ::: "memory");
    if (b == 255 && lane == 0) __builtin_amdgcn_raw_buffer_store_b32(tag, rws, 0, WS_EPOCH, AUX_SC1);
    if (f1 < 172) {
      auto stage = [&]() {
        unsigned an = ~0u, orr = 0u;
#pragma unroll
        for (int i = 0; i < 4; ++i) {
          PIN(g[i]);
          LDS_U2(st_w + i * 512) = (v2u){g[i].y, g[i].w};
          an &= g[i].x & g[i].z; orr |= g[i].x | g[i].z;
        }
        PIN(an); PIN(orr);
        return __all(an == tag && orr == tag);
      };
      if (__builtin_expect(!stage(), 0)) {
#pragma unroll 1
        for (unsigned s = 0; s < SPIN_CAP; ++s) {
          FENCE_();
#pragma unroll
          for (int i = 0; i < 4; ++i) g[i] = __builtin_amdgcn_raw_buffer_load_b128(rws, l16 + (i & 1) * 1024, i < 2 ? r1 : r2, AUX_SC1);
          if (stage()) break;
        }
      }
      WAIT_LGKM();
      v4u bb[16];
#pragma unroll
      for (int i = 0; i < 16; ++i) bb[i] = LDS_U4(st_r + i * 64);
      SB();
      f32x4 d0 = {0.f, 0.f, 0.f, 0.f}, d1 = d0;
#pragma unroll
      for (int i = 0; i < 16; i += 2) { d0 = mfma16(a[i], bb[i], d0); d1 = mfma16(a[i + 1], bb[i + 1], d1); }
      d0 += d1;
      float av[4];
#pragma unroll
      for (int j = 0; j < 4; ++j) {
        auto r = __builtin_amdgcn_permlane32_swap(asu(d0[j]), asu(d0[j]), false, false);
        const unsigned r0 = r[0], r1_ = r[1];
        const float gs = rbf(asf(r0)), us = rbf(asf(r1_));
        av[j] = rbf(gs * __builtin_amdgcn_rcpf(1.f + __builtin_amdgcn_exp2f(gs * -1.4426950408889634f))) * us;
      }
      if (lane < 32 && r16 < 4)
        __builtin_amdgcn_raw_buffer_store_b128((v4u){tag, pk2(av[0], av[1]), tag, pk2(av[2], av[3])}, rws, r16 * 32 + G * 16, WS_AGRAN + f1 * 128, AUX_SC1);
    } else if (f2 >= 0) {
      const unsigned ho = ((G & 1) ? r2 : r1) + (8 * f2 + (r16 >> 1)) * 8;
      const V2h hg = poll<V2h>([&]() { return V2h{__builtin_amdgcn_raw_buffer_load_b64(rws, ho, 0, AUX_SC1), __builtin_amdgcn_raw_buffer_load_b64(rws, ho + 2048, 0, AUX_SC1)}; },
                               [&](const V2h& v) { return v.x.x == tag && v.y.x == tag; });
      float hnv = (lane & 1) ? bfhi(hg.x.y) : bflo(hg.x.y), hval = (lane & 1) ? bfhi(hg.y.y) : bflo(hg.y.y);
      const unsigned Ks = (G & 1) ? K2 : K1, ordk = Ks >> 11;
      const unsigned rb = (ordk & 0x8000u) ? (ordk & 0x7fffu) : (~ordk & 0xffffu);
      float sg = rbf(__builtin_amdgcn_rcpf(1.f + __builtin_amdgcn_exp2f(bflo(rb) * -1.4426950408889634f)));
      unsigned a_rr = sb + L_PRIV + 8192 + (G & 1) * 64 + r16 * 4, oout = (2047u - (Ks & 2047u)) * 1024 + (16 * f2 + r16) * 2;
      const __amdgpu_buffer_rsrc_t rout = __builtin_amdgcn_make_buffer_rsrc(p.out, 0, 2048 * 1024, 0x00020000);
      PIN(hnv); PIN(hval); PIN(sg); PIN(a_rr); PIN(oout);
      SB();
      RBAR();
      float y = (LDS_F1(a_rr) + LDS_F1(a_rr + 128)) + (LDS_F1(a_rr + 256) + LDS_F1(a_rr + 384));
      y = rbf(rbf(y) * sg);
      const float t = rbf(hnv + y);
      const unsigned ov = pk2(hval + t, 0.f) & 0xffffu;
      const unsigned nb = dppu<0xB1>(ov);
      if (lane < 32 && (lane & 1) == 0) __builtin_amdgcn_raw_buffer_store_b32(ov | (nb << 16), rout, oout, 0, 0);
    }
    asm volatile(".Lfb_code2:");
    return;
  }

  const __amdgpu_buffer_rsrc_t rws = __builtin_amdgcn_make_buffer_rsrc(p.ws, 0, WS_BYTES, 0x00020000);
  const int tb = w & 1, ct = w >> 1;
  unsigned so_kv = WS_KV + myblk * 16384;
  SPIN_(so_kv);
  unsigned xa[4], wa[4], wb[4];
#pragma unroll
  for (int i = 0; i < 4; ++i) {
    const int ks = i ^ (ct | (tb << 1));
    xa[i] = sb + L_X + (tb * 32 + r32) * 128 + (((2 * ks + h) ^ ((r32 >> 1) & 7)) << 4);
    wa[i] = sb + L_RING + (16 * ks + 8 * h + q4) * 128 + ((ct ^ ((q4 >> 1) & 1)) << 6) + (G & 1) * 32 + p4 * 8;
    wb[i] = wa[i] + 49152;
    PIN(xa[i]); PIN(wa[i]); PIN(wb[i]);
  }
  unsigned a_csw = sb + L_CS + h * 512 + w * 128 + r32 * 4, a_xsw = sb + L_XS + w * 256 + r32 * 8 + h * 4, a_cqw = sb + L_CS + 1024 + w * 128 + r32 * 4;
  unsigned a_tl = sb + L_TAB + (tb * 32 + r32) * 4, a_tr = sb + L_TAB + tb * 128 + h * 16;
  unsigned a_cl = sb + L_TAB + 768 + (ct * 32 + r32) * 4, a_cr4 = sb + L_TAB + 512 + ct * 128 + h * 16;
  unsigned ko = tb * 8192 + 2 * ct * 1024 + lane * 16;
  unsigned vo = tb * 8192 + 4096 + (2 * ct + (r32 >> 4)) * 1024 + (h * 32 + r16) * 16;
  unsigned a_kx = sb + L_KVX + ko, a_vx = sb + L_KVX + vo, a_qw = sb + L_QX + (tb * 4 + 2 * ct) * 1024 + lane * 16;
  unsigned a_xres = sb + L_X + ((gg - rot) & 7) * 8192 + (w * 16 + r16) * 128 + (((G >> 1) ^ ((r16 >> 1) & 7)) << 4) + (G & 1) * 8;
  PIN(a_csw); PIN(a_xsw); PIN(a_cqw); PIN(a_tl); PIN(a_tr); PIN(a_cl); PIN(a_cr4);
  PIN(ko); PIN(vo); PIN(a_kx); PIN(a_vx); PIN(a_qw); PIN(a_xres);
  f32x16 acc0, acc1;
#pragma unroll
  for (int i = 0; i < 16; ++i) { acc0[i] = 0.f; acc1[i] = 0.f; }
  auto rd = [&](Frags& f, int slab, int sl) {
#pragma unroll
    for (int i = 0; i < 4; ++i) {
      const unsigned wo = sl < 3 ? wa[i] + sl * 16384 : wb[i] + (sl - 3) * 16384;
      f.x[i] = LDS_U4(xa[i] + slab * 8192); f.a[i] = tr16x2(wo, 512); f.b[i] = tr16x2(wo + 8192, 512);
    }
  };
  auto mix = [&]() {
    PIN(acc0); PIN(acc1);
#pragma unroll
    for (int i = 0; i < 8; ++i) { __builtin_amdgcn_sched_group_barrier(0x8, 1, 0); __builtin_amdgcn_sched_group_barrier(0x100, 3, 0); }
    SB();
  };
  auto mmA = [&](const Frags& f) {
#pragma unroll
    for (int i = 0; i < 4; ++i) { acc0 = mfma32(f.a[i], f.x[i], acc0); acc1 = mfma32(f.x[i], f.b[i], acc1); }
  };
  Frags fa, fb2;
  PIN(acc0); PIN(acc1);
  BAR();
  rd(fa, 0, 0);
  {
    const v4u q0 = tr16x2(wb[0] + (L_QX - L_RING - 49152), 512), q1 = tr16x2(wb[1] + (L_QX - L_RING - 49152), 512);
    SB();
    float cs0 = 0.f, cs1 = 0.f, cs2 = 0.f, xs = 0.f, xq = 0.f;
#pragma unroll
    for (int j = 0; j < 4; ++j) {
      cs0 = dot2(fa.a[0][j], ONE2, cs0); cs1 = dot2(fa.b[0][j], ONE2, cs1);
      xs = dot2(fa.x[0][j], ONE2, xs); xq = dot2(fa.x[0][j], fa.x[0][j], xq);
    }
#pragma unroll
    for (int j = 0; j < 4; ++j) {
      cs0 = dot2(fa.a[1][j], ONE2, cs0); cs1 = dot2(fa.b[1][j], ONE2, cs1);
      xs = dot2(fa.x[2][j], ONE2, xs); xq = dot2(fa.x[2][j], fa.x[2][j], xq);
    }
#pragma unroll
    for (int j = 0; j < 4; ++j) { cs2 = dot2(q0[j], ONE2, cs2); cs2 = dot2(q1[j], ONE2, cs2); }
    auto rc = __builtin_amdgcn_permlane32_swap(asu(cs0), asu(cs1), false, false);
    auto rx = __builtin_amdgcn_permlane32_swap(asu(xs), asu(xq), false, false);
    const unsigned c0 = rc[0], c1 = rc[1], x0 = rx[0], x1 = rx[1];
    LDS_F1(a_csw) = asf(c0) + asf(c1);
    LDS_F1(a_xsw) = asf(x0) + asf(x1);
    LDS_F1(a_cqw) = sum32(cs2);
  }
  BAR(); rd(fb2, 1, 1); mmA(fa); mix();
  BAR(); rd(fa, 2, 2); mmA(fb2); mix();
  BAR(); rd(fb2, 3, 3); mmA(fa); mix();
  BAR(); rd(fa, 4, 4); mmA(fb2); mix();
  BAR(); rd(fb2, 5, 0); mmA(fa); mix();
  BAR(); rd(fa, 6, 1); mmA(fb2); mix(); mmA(fa); PIN(acc0); PIN(acc1);
  BAR(); rd(fb2, 7, 2); SB();
#pragma unroll
  for (int i = 0; i < 4; ++i) acc0 = mfma32(fb2.a[i], fb2.x[i], acc0);
  PIN(acc0);
  BAR();
  float rs1, mr1;
  v4u xh[4];
  auto rdx = [&](int slab) {
#pragma unroll
    for (int i = 0; i < 4; ++i) xh[i] = LDS_U4(xa[i] + slab * 8192);
  };
  {
    rs1 = LDS_F1(a_tl); mr1 = LDS_F1(a_tl + 256);
    f32x4 ck[4], r4[4], m4[4];
#pragma unroll
    for (int g = 0; g < 4; ++g) ck[g] = LDS_F4(a_cr4 + g * 32);
    const float cv = LDS_F1(a_cl);
#pragma unroll
    for (int g = 0; g < 4; ++g) { r4[g] = LDS_F4(a_tr + g * 32); m4[g] = LDS_F4(a_tr + 256 + g * 32); }
#pragma unroll
    for (int i = 0; i < 4; ++i) acc1 = mfma32(fb2.x[i], fb2.b[i], acc1);
    PIN(acc1);
#pragma unroll
    for (int i = 0; i < 4; ++i) { __builtin_amdgcn_sched_group_barrier(0x8, 1, 0); __builtin_amdgcn_sched_group_barrier(0x100, 4, 0); }
    SB();
    float o[16];
#pragma unroll
    for (int g = 0; g < 4; ++g)
#pragma unroll
      for (int j = 0; j < 4; ++j) o[4 * g + j] = fmaf(acc0[4 * g + j], rs1, mr1 * ck[g][j]);
    const v4u k0 = pack8(o), k1 = pack8(o + 8);
    __builtin_amdgcn_raw_buffer_store_b128(k0, rws, ko, so_kv, AUX_SC1);
    __builtin_amdgcn_raw_buffer_store_b128(k1, rws, ko + 1024, so_kv, AUX_SC1);
    SB();
#pragma unroll
    for (int g = 0; g < 4; ++g)
#pragma unroll
      for (int j = 0; j < 4; ++j) o[4 * g + j] = fmaf(acc1[4 * g + j], r4[g][j], m4[g][j] * cv);
    const v4u v0 = pack8(o), v1 = pack8(o + 8);
    __builtin_amdgcn_raw_buffer_store_b128(v0, rws, vo, so_kv, AUX_SC1);
    __builtin_amdgcn_raw_buffer_store_b128(v1, rws, vo + 256, so_kv, AUX_SC1);
    SB();
    rd(fa, 0, 3); rdx(1);
    LDS_U4(a_kx) = k0; LDS_U4(a_kx + 1024) = k1;
    LDS_U4(a_vx) = v0; LDS_U4(a_vx + 256) = v1;
    SB();
  }
#pragma unroll
  for (int i = 0; i < 16; ++i) acc0[i] = 0.f;
  auto mmB = [&](const Frags& f) {
#pragma unroll
    for (int i = 0; i < 4; ++i) acc0 = mfma32(f.b[i], xh[i], acc0);
#pragma unroll
    for (int i = 0; i < 4; ++i) acc0 = mfma32(f.a[i], f.x[i], acc0);
  };
  RBAR();
  rd(fb2, 2, 4); mmB(fa); rdx(3); mix();
  mmB(fb2);
  PIN(acc0);
  SB();
  WAIT_VM(0);
  BAR();
  rd(fa, 4, 0); rdx(5);
  SB();
  rd(fb2, 6, 1); mmB(fa); rdx(7); mix();
  v2u xres[4];
  {
    f32x4 cq[4];
#pragma unroll
    for (int g = 0; g < 4; ++g) cq[g] = LDS_F4(a_cr4 + 512 + g * 32);
#pragma unroll
    for (int c = 0; c < 4; ++c) xres[c] = LDS_U2(a_xres ^ (c * 32));
    mmB(fb2);
    PIN(acc0);
    float t[16];
#pragma unroll
    for (int i = 0; i < 16; ++i) { t[i] = mr1 * cq[i >> 2][i & 3]; PIN(t[i]); }
    SB();
    float o[16];
    const float sc = 0.25f * 1.4426950408889634f;
#pragma unroll
    for (int i = 0; i < 16; ++i) o[i] = fmaf(acc0[i], rs1, t[i]);
    const v4u a0 = pack8(o), a1 = pack8(o + 8);
#pragma unroll
    for (int j = 0; j < 4; ++j) { o[2 * j] = bflo(a0[j]) * sc; o[2 * j + 1] = bfhi(a0[j]) * sc; o[8 + 2 * j] = bflo(a1[j]) * sc; o[9 + 2 * j] = bfhi(a1[j]) * sc; }
    LDS_U4(a_qw) = pack8(o);
    LDS_U4(a_qw + 1024) = pack8(o + 8);
  }
  BAR();
  const unsigned tag = read_tag(p.ws);
  const int tok = tt * 64 + w * 16 + r16;
  unsigned so_ofr = WS_OFLAG + tt * 1024;
  unsigned so_st = WS_STATS + (tt * 4 + w) * 2048, so_hr = WS_HROWS + (tt * 4 + w) * 8192 + gg * 256;
  unsigned oq[4];
#pragma unroll
  for (int qn = 0; qn < 4; ++qn) { oq[qn] = WS_O + (tt * 4 + w) * 16384 + ((qn + tt) & 3) * 4096; SPIN_(oq[qn]); }
  SPIN_(so_ofr); SPIN_(so_st); SPIN_(so_hr);
  unsigned woa[4], wob[4];
#pragma unroll
  for (int c = 0; c < 4; ++c) {
    woa[c] = sb + L_RING + (16 * (G >> 1) + 4 * (G & 1) + q4) * 128 + ((c ^ (2 * (G & 1) + (q4 >> 1))) << 5) + p4 * 8;
    wob[c] = woa[c] + 49152;
    PIN(woa[c]); PIN(wob[c]);
  }
  auto wofrag = [&](int qn, int ks, int c) { return tr16x2((qn < 2 ? wob[c] + qn * 16384 : woa[c] + (qn - 2) * 16384) + ks * 4096, 1024); };
  v2u wr[4];
#pragma unroll
  for (int c = 0; c < 4; ++c) { wr[c] = LDS_U2(sb + L_WR + gg * 128 + G * 8 + c * 32); PIN(wr[c]); }
  const v4u wrall = LDS_U4(sb + L_WR + lane * 16);
  float wrsum;
  {
    float s = 0.f;
#pragma unroll
    for (int j = 0; j < 4; ++j) s = dot2(wrall[j], ONE2, s);
    wrsum = sum32(sum16(fsum_row16(s)));
    PIN(wrsum);
  }
  const __amdgpu_buffer_rsrc_t rout = __builtin_amdgcn_make_buffer_rsrc(p.out, 0, 2048 * 1024, 0x00020000);
  const unsigned wpriv = sb + L_PRIV + w * 2048;
  unsigned l16 = lane * 16, ofo = (lane & 7) * 128, sto = lane * 16 + gg * 256;
  unsigned key, a_cw = wpriv + G * 8, a_cr = wpriv + lane * 8, hro = G * 2048 + r16 * 16, keylo = 2047 - tok;
  unsigned fv = f2 >= 0 ? (unsigned)((lane >> 1) * 1024 + (lane & 1) * 16) : (unsigned)lane * (FF * 2);
  PIN(l16); PIN(ofo); PIN(sto); PIN(a_cw); PIN(a_cr); PIN(hro); PIN(keylo); PIN(fv);
  SB();
  BAR();
  auto ld_wd = [&](int j0, int j1) {
    if (f2 >= 0) {
#pragma unroll 1
      for (int j = j0; j < j1; ++j) {
        int i = w + 4 * (j + 3 * (f2 & 3));
        i = i >= 44 ? i - 44 : i;
        if (i < 43) glds1(fv, p.wd + f2 * 32 + (size_t)i * 32 * 1024, sb + L_FFW + i * 1024);
      }
    }
  };
  auto ld_gu = [&](int j) {
    const int i = (j * 4 + w + 2 * f1) & 15;
    if (f1 < 172) glds1(fv, ((i & 1) ? p.wu : p.wg) + f1 * 16 + (size_t)(i >> 1) * 64 * FF * 2, sb + L_FFW + (i & 1) * 8192 + (i >> 1) * 1024);
  };
  issue_wo(0); ld_gu(0); issue_wo(1); issue_wo(2); issue_wo(3);
  if (slot >= 26) {
#pragma unroll 1
    for (int u = (xcd * 6 + slot - 26) * 4 + w; u < 1376; u += 192) glds1(l16, p.wd + (size_t)u * 1024, sb + L_JUNK);
  }
  WAIT_VM(0);
  RBAR();
  v4u wf[2][4][4];
#pragma unroll
  for (int qn = 0; qn < 2; ++qn)
#pragma unroll
    for (int ks = 0; ks < 4; ++ks)
#pragma unroll
      for (int c = 0; c < 4; ++c) { wf[qn][ks][c] = wofrag(qn, ks, c); if (qn == 0 && ks == 1 && c == 0) { SB(); ld_gu(1); ld_wd(0, 2); } }
  SB();
  float hv[16];
  unsigned hb[8];
  {
    __builtin_amdgcn_s_sleep(OSLEEP);
    poll<unsigned>([&]() { return __builtin_amdgcn_raw_buffer_load_b32(rws, ofo, so_ofr, AUX_SC1); }, [&](unsigned f) { return f == tag; });
    v4u of[16];
    auto ldo = [&](int k) { of[k] = __builtin_amdgcn_raw_buffer_load_b128(rws, l16 + (k & 3) * 1024, oq[k >> 2], AUX_SC1); SB(); };
#pragma unroll
    for (int k = 0; k < 8; ++k) ldo(k);
    f32x4 a[4];
#pragma unroll
    for (int c = 0; c < 4; ++c) a[c] = (f32x4){0.f, 0.f, 0.f, 0.f};
#pragma unroll
    for (int k = 0; k < 16; ++k) {
#pragma unroll
      for (int c = 0; c < 4; ++c) {
        a[c] = mfma16(wf[(k >> 2) & 1][k & 3][c], of[k], a[c]);
        PIN(a[c]);
        if (k < 8) wf[k >> 2][k & 3][c] = wofrag((k >> 2) + 2, k & 3, c);
        SB();
      }
      if (k + 8 < 16) ldo(k + 8);
    }
#pragma unroll
    for (int c = 0; c < 4; ++c)
#pragma unroll
      for (int m = 0; m < 2; ++m) {
        const unsigned xr = xres[c][m];
        const unsigned ab = pk2(a[c][2 * m], a[c][2 * m + 1]);
        hb[2 * c + m] = pk2(bflo(xr) + bflo(ab), bfhi(xr) + bfhi(ab));
      }
    float s1 = 0.f, s2 = 0.f, s3 = 0.f;
#pragma unroll
    for (int m = 0; m < 8; ++m) { s1 = dot2(hb[m], ONE2, s1); s2 = dot2(hb[m], hb[m], s2); s3 = dot2(hb[m], wr[m >> 1][m & 1], s3); }
    s1 = sum32(sum16(s1)); s2 = sum32(sum16(s2)); s3 = sum32(sum16(s3));
    if (lane < 16)
      __builtin_amdgcn_raw_buffer_store_b128((v4u){asu(s1), asu(s2), asu(s3), tag ^ asu(s1) ^ asu(s2) ^ asu(s3)}, rws, sto, so_st, AUX_SC1);
    SB();
#pragma unroll
    for (int m = 0; m < 8; ++m) { hv[2 * m] = bflo(hb[m]); hv[2 * m + 1] = bfhi(hb[m]); PIN(hv[2 * m]); PIN(hv[2 * m + 1]); }
  }
  ld_gu(2);
  ld_wd(2, 6);
  ld_gu(3);
  v4u out0, out1;
  bool cand0, cand1;
  const unsigned tok0 = tt * 64 + w * 16 + (lane >> 3), oo = tok0 * 1024 + gg * 128 + (lane & 7) * 16;
  {
    float s1, s2, s3;
    __builtin_amdgcn_s_sleep(SSLEEP);
    {
      const V2 r = poll<V2>([&]() { return V2{__builtin_amdgcn_raw_buffer_load_b128(rws, l16, so_st, AUX_SC1), __builtin_amdgcn_raw_buffer_load_b128(rws, l16 + 1024, so_st, AUX_SC1)}; },
                             [&](const V2& v) { return (((v.a.x ^ v.a.y ^ v.a.z ^ v.a.w) ^ tag) | ((v.c.x ^ v.c.y ^ v.c.z ^ v.c.w) ^ tag)) == 0u; });
      s1 = asf(r.a.x) + asf(r.c.x); s2 = asf(r.a.y) + asf(r.c.y); s3 = asf(r.a.z) + asf(r.c.z);
    }
    s1 = sum32(sum16(s1)); s2 = sum32(sum16(s2)); s3 = sum32(sum16(s3));
    float mb, rs;
    ln_scalars(s1, s2, mb, rs);
    const unsigned rb = pk2((s3 - mb * wrsum) * rs, 0.f) & 0xffffu;
    const unsigned ord = (rb & 0x8000u) ? (~rb & 0xffffu) : (rb | 0x8000u);
    key = (ord << 11) | keylo;
    const unsigned k1 = umax_row16(key);
    const unsigned k2 = umax_row16(key == k1 ? 0u : key);
    if (lane == 0) LDS_U2(sb + L_KEY + w * 8) = (v2u){k1, k2};
    WAIT_VM(0);
    BAR();
    unsigned hn[8];
#pragma unroll
    for (int m = 0; m < 8; ++m) {
      const unsigned d = pk2(hv[2 * m] - mb, hv[2 * m + 1] - mb);
      hn[m] = pk2(bflo(d) * rs, bfhi(d) * rs);
    }
    if (key == k1 || key == k2) {
      const unsigned base = a_cw + (key == k1 ? 0 : 256);
#pragma unroll
      for (int c = 0; c < 4; ++c) {
        LDS_U2(base + c * 32) = (v2u){hn[2 * c], hn[2 * c + 1]};
        LDS_U2(base + 128 + c * 32) = (v2u){hb[2 * c], hb[2 * c + 1]};
      }
    }
    WAIT_LGKM();
    {
      const v2u e = LDS_U2(a_cr);
      __builtin_amdgcn_raw_buffer_store_b128((v4u){tag, e.x, tag, e.y}, rws, hro, so_hr, AUX_SC1);
    }
    SB();
    ld_wd(6, 11);
    unsigned ob[8];
#pragma unroll
    for (int m = 0; m < 8; ++m) ob[m] = pk2(hv[2 * m] + bflo(hn[m]), hv[2 * m + 1] + bfhi(hn[m]));
    {
      const unsigned ow = wpriv + r16 * 128 + ((G >> 1) ^ (r16 & 7)) * 16 + (G & 1) * 8;
#pragma unroll
      for (int c = 0; c < 4; ++c) LDS_U2(ow ^ (c * 32)) = (v2u){ob[2 * c], ob[2 * c + 1]};
      WAIT_LGKM();
      const unsigned orr = wpriv + (lane >> 3) * 128 + ((lane & 7) ^ (lane >> 3)) * 16;
      out0 = LDS_U4(orr); out1 = LDS_U4(orr + 1024);
      PIN(out0); PIN(out1);
    }
    const unsigned c1 = 2047u - (k1 & 2047u), c2 = 2047u - (k2 & 2047u);
    cand0 = tok0 == c1 || tok0 == c2; cand1 = tok0 + 8 == c1 || tok0 + 8 == c2;
    if (f2 >= 0) cand0 = cand1 = true;
    {
      __builtin_amdgcn_s_sleep(OUTEARLY);
      if (!cand0) __builtin_amdgcn_raw_buffer_store_b128(out0, rout, oo, 0, AUX_SC1);
      if (!cand1) __builtin_amdgcn_raw_buffer_store_b128(out1, rout, oo + 8192, 0, AUX_SC1);
    }
  }
  auto rest_out = [&]() {
    const v2u kk = LDS_U2(sb + L_SEL);
    const unsigned t1 = 2047u - (kk.x & 2047u), t2 = 2047u - (kk.y & 2047u);
    if (cand0 && tok0 != t1 && tok0 != t2) __builtin_amdgcn_raw_buffer_store_b128(out0, rout, oo, 0, AUX_SC1);
    if (cand1 && tok0 + 8 != t1 && tok0 + 8 != t2) __builtin_amdgcn_raw_buffer_store_b128(out1, rout, oo + 8192, 0, AUX_SC1);
  };
  v4u wd[11];
  if (f2 >= 0) {
    const int ksC = w + 4 * (G + 8);
    unsigned oc = (ksC < 43 ? ksC : w) * 512 + q4 * 128 + p4 * 16, oab = (w + 4 * G) * 512 + q4 * 128 + p4 * 16;
    unsigned a_b = wpriv + G * 32 + (lane & 1) * 16, a_rw = sb + L_PRIV + 8192 + (w * 2 + r16) * 64 + G * 16;
    PIN(oc); PIN(oab); PIN(a_b); PIN(a_rw);
    WAIT_VM(0);
    const unsigned wa2 = sb + L_FFW + (32 * w + 8 * G + q4) * 32 + p4 * 8;
#pragma unroll
    for (int i = 0; i < 11; ++i) wd[i] = tr16x2(wa2 + ((i == 10 && w == 3) ? 0 : i * 4096), 128);
    if (w == 3) wd[10] = (v4u){0u, 0u, 0u, 0u};
#pragma unroll
    for (int i = 0; i < 11; ++i) PIN(wd[i]);
    RBAR();
    rest_out();
    __builtin_amdgcn_s_sleep(ASLEEP);
    const V3 gr = poll<V3>([&]() { return V3{__builtin_amdgcn_raw_buffer_load_b128(rws, oab, WS_AGRAN, AUX_SC1), __builtin_amdgcn_raw_buffer_load_b128(rws, oab, WS_AGRAN + 8192, AUX_SC1),
                                             __builtin_amdgcn_raw_buffer_load_b128(rws, oc, WS_AGRAN, AUX_SC1)}; },
                           [&](const V3& v) { return ((v.a.x ^ tag) | (v.a.z ^ tag) | (v.b.x ^ tag) | (v.b.z ^ tag) | (v.c.x ^ tag) | (v.c.z ^ tag)) == 0u; });
    LDS_U2(a_cr) = (v2u){gr.a.y, gr.a.w};
    LDS_U2(a_cr + 512) = (v2u){gr.b.y, gr.b.w};
    LDS_U2(a_cr + 1024) = (v2u){gr.c.y, gr.c.w};
    WAIT_LGKM();
    v4u bb[11];
#pragma unroll
    for (int i = 0; i < 11; ++i) bb[i] = LDS_U4(a_b + i * 128);
    SB();
    f32x4 d0 = {0.f, 0.f, 0.f, 0.f}, d1 = d0;
#pragma unroll
    for (int i = 0; i < 11; ++i) { if (i & 1) d1 = mfma16(wd[i], bb[i], d1); else d0 = mfma16(wd[i], bb[i], d0); }
    d0 += d1;
    if (r16 < 2) LDS_F4(a_rw) = d0;
    BAR();
  } else {
    RBAR();
    rest_out();
  }
  asm volatile(".Lfb_code1:");
}
}

extern "C" void kernel_launch(void* const* d_in, const int* in_sizes, int n_in, void* d_out, int out_size, void* d_ws,
                              size_t ws_size, hipStream_t stream) {
  Params p;
  p.x = (const char*)d_in[0]; p.wqkv = (const char*)d_in[1]; p.wo = (const char*)d_in[2];
  p.wr = (const char*)d_in[4]; p.wg = (const char*)d_in[5]; p.wu = (const char*)d_in[6]; p.wd = (const char*)d_in[7];
  p.out = (char*)d_out; p.ws = (char*)d_ws;
  (void)hipFuncSetAttribute((const void*)fused_block_kernel, hipFuncAttributeMaxDynamicSharedMemorySize, L_TOTAL);
  hipLaunchKernelGGL(fused_block_kernel, dim3(256), dim3(512), L_TOTAL, stream, p);
}
```
